```python
import torch

def _softmax(x, axis=-1):
    return torch.softmax(x, dim=axis)
def _gelu(x):
    return torch.nn.functional.gelu(x, approximate="tanh")
def _silu(x):
    return torch.nn.functional.silu(x)
def _sigmoid(x):
    return torch.sigmoid(x)
def _layernorm(x, axis=-1, eps=1e-5):
    m = x.mean(dim=axis, keepdim=True)
    v = x.var(dim=axis, keepdim=True, unbiased=False)
    return (x - m) / torch.sqrt(v + eps)
def _scatter_add(tgt, idx, src, axis=0):
    return tgt.clone().index_add_(axis, idx, src)
def _segment_sum(src, seg, n):
    out = torch.zeros((n,) + src.shape[1:], dtype=src.dtype)
    return out.index_add_(0, seg, src)
def _pad(x, pw):
    flat = tuple(v for pair in reversed(pw) for v in pair)
    return torch.nn.functional.pad(x, flat)

kernel_name = 'concat_heads__ssm_conv_s5__grouped_kv_mqa__prompt'

def setup_inputs():

    _w = (lambda a: (lambda t: t.to(torch.bfloat16) if t.is_floating_point() else t)(torch.as_tensor(a)))
    def _A_setup():
        B, S, D = 1, 2048, 512
        P = D
        Lambda_re = -0.5 * (lambda _x: torch.exp(torch.as_tensor(_x)))((lambda *s: torch.randn(*s, dtype=torch.bfloat16) * 0.2)(P))
        Lambda_im = (lambda *s: torch.randn(*s, dtype=torch.bfloat16) * 0.4)(P) * 1.0
        B_re = (lambda *s: torch.randn(*s, dtype=torch.bfloat16) * 0.2)(P, D) * (D ** -0.5)
        B_im = (lambda *s: torch.randn(*s, dtype=torch.bfloat16) * 0.2)(P, D) * (D ** -0.5)
        C_re = (lambda *s: torch.randn(*s, dtype=torch.bfloat16) * 0.2)(D, P) * (P ** -0.5)
        C_im = (lambda *s: torch.randn(*s, dtype=torch.bfloat16) * 0.2)(D, P) * (P ** -0.5)
        log_dt = (lambda *s: torch.randn(*s, dtype=torch.bfloat16) * 0.2)(P) * 0.3 - 2.0
        Dskip = (lambda *s: torch.randn(*s, dtype=torch.bfloat16) * 0.05)(D)
        return {
            "x": _w((lambda *s: torch.randn(*s, dtype=torch.bfloat16) * 0.05)(B, S, D)),
            "Lambda_re": _w(Lambda_re),
            "Lambda_im": _w(Lambda_im),
            "B_re": _w(B_re),
            "B_im": _w(B_im),
            "C_re": _w(C_re),
            "C_im": _w(C_im),
            "log_dt": _w(log_dt),
            "Dskip": _w(Dskip),
        }
    _A_in = _A_setup()

    def _B_setup():
        B, S, D, H = 1, 2048, 512, 32
        Dh = D // H
        return {
            "x": _w((lambda *s: torch.randn(*s, dtype=torch.bfloat16) * 0.05)(B, S, D)),
            "wq": _w((lambda *s: torch.randn(*s, dtype=torch.bfloat16) * 0.8)(D, H * Dh)),
            "wk": _w((lambda *s: torch.randn(*s, dtype=torch.bfloat16) * 0.8)(D, Dh)),
            "wv": _w((lambda *s: torch.randn(*s, dtype=torch.bfloat16) * 0.05)(D, Dh)),
            "wo": _w((lambda *s: torch.randn(*s, dtype=torch.bfloat16) * 0.05)(H * Dh, D)),
        }
    _B_in = _B_setup()

    return {
        "A_x": _A_in["x"],
        "A_Lambda_re": _A_in["Lambda_re"],
        "A_Lambda_im": _A_in["Lambda_im"],
        "A_B_re": _A_in["B_re"],
        "A_B_im": _A_in["B_im"],
        "A_C_re": _A_in["C_re"],
        "A_C_im": _A_in["C_im"],
        "A_log_dt": _A_in["log_dt"],
        "A_Dskip": _A_in["Dskip"],
        "B_x": _B_in["x"],
        "B_wq": _B_in["wq"],
        "B_wk": _B_in["wk"],
        "B_wv": _B_in["wv"],
        "B_wo": _B_in["wo"],
    }

def reference(A_x, A_Lambda_re, A_Lambda_im, A_B_re, A_B_im, A_C_re, A_C_im, A_log_dt, A_Dskip, B_x, B_wq, B_wk, B_wv, B_wo):
    def _A_ref(x, Lambda_re, Lambda_im, B_re, B_im, C_re, C_im, log_dt, Dskip):
        Bsz, L, H = x.shape
        Lam = Lambda_re + 1j * Lambda_im
        Bmat = B_re + 1j * B_im
        Cmat = C_re + 1j * C_im
        dt = torch.exp(log_dt)
        Lam_bar = torch.exp(dt * Lam)
        B_bar = ((Lam_bar - 1.0) / Lam)[:, None] * Bmat
        Bu = torch.einsum("bld,pd->blp", x + 0j, B_bar)
        h = torch.zeros_like(Bu[:, 0, :])
        hs = []
        for t in range(L):
            h = Lam_bar[None, :] * h + Bu[:, t, :]
            hs.append(h)
        hseq = torch.stack(hs, 1)
        y = 2.0 * torch.einsum("blp,dp->bld", hseq, Cmat).real + x * Dskip[None, None, :]
        return y

    def _B_ref(x, wq, wk, wv, wo):
        B, S, D = x.shape
        H = 32
        Dh = D // H
        q = (lambda x, *d: x.permute(*d))((x @ wq).reshape(B, S, H, Dh), 0, 2, 1, 3)
        k = x @ wk
        v = x @ wv
        scores = torch.einsum("bhqd,bkd->bhqk", q, k) * (Dh ** -0.5)
        p = _softmax(scores, axis=-1)
        o = torch.einsum("bhqk,bkd->bhqd", p, v)
        return (lambda x, *d: x.permute(*d))(o, 0, 2, 1, 3).reshape(B, S, H * Dh) @ wo

    a = _A_ref(A_x, A_Lambda_re, A_Lambda_im, A_B_re, A_B_im, A_C_re, A_C_im, A_log_dt, A_Dskip)
    b = _B_ref(B_x, B_wq, B_wk, B_wv, B_wo)
    return torch.concatenate([a, b], axis=-1)
```

```cpp
#include <hip/hip_runtime.h>
#include <hip/hip_bf16.h>
#include <stdint.h>

typedef float f32x16 __attribute__((ext_vector_type(16)));
typedef float f32x8 __attribute__((ext_vector_type(8)));
typedef float f32x4 __attribute__((ext_vector_type(4)));
typedef float f32x2 __attribute__((ext_vector_type(2)));
typedef unsigned u32x4 __attribute__((ext_vector_type(4)));
typedef unsigned u32x2 __attribute__((ext_vector_type(2)));
typedef short s16x4 __attribute__((ext_vector_type(4)));
typedef __bf16 bf16x2 __attribute__((ext_vector_type(2)));
typedef __bf16 bf16x8 __attribute__((ext_vector_type(8)));
typedef __bf16 bf16x16 __attribute__((ext_vector_type(16)));
typedef __amdgpu_buffer_rsrc_t rsrc_t;
#define DI __device__ __forceinline__
#define LDSP __attribute__((address_space(3)))
#define CONSTP __attribute__((address_space(4)))
typedef unsigned u32x8 __attribute__((ext_vector_type(8)));

#define ATTN_PRO \
  "buffer_load_dwordx4 v[192:195], v240, %[srd], 0 offen sc1\n" \
  "buffer_load_dwordx4 v[196:199], v241, %[srd], 0 offen offset:1024 sc1\n" \
  "buffer_load_dwordx4 v[200:203], v241, %[srd], 0 offen offset:1568 sc1\n" \
  ""
#define ATTN_LOOP \
  "s_mov_b32 %[so], 2112\n" \
  "buffer_load_dwordx4 v[204:207], v240, %[srd], %[so] offen sc1\n" \
  "v_pk_mov_b32 v[32:33], 0, 0\n" \
  "v_pk_mov_b32 v[34:35], 0, 0\n" \
  "v_pk_mov_b32 v[36:37], 0, 0\n" \
  "v_pk_mov_b32 v[38:39], 0, 0\n" \
  "v_pk_mov_b32 v[40:41], 0, 0\n" \
  "v_pk_mov_b32 v[42:43], 0, 0\n" \
  "v_pk_mov_b32 v[44:45], 0, 0\n" \
  "v_pk_mov_b32 v[46:47], 0, 0\n" \
  "buffer_load_dwordx4 v[208:211], v241, %[srd], %[so] offen offset:1024 sc1\n" \
  "v_pk_mov_b32 v[48:49], 0, 0\n" \
  "v_pk_mov_b32 v[50:51], 0, 0\n" \
  "v_pk_mov_b32 v[52:53], 0, 0\n" \
  "v_pk_mov_b32 v[54:55], 0, 0\n" \
  "v_pk_mov_b32 v[56:57], 0, 0\n" \
  "v_pk_mov_b32 v[58:59], 0, 0\n" \
  "v_pk_mov_b32 v[60:61], 0, 0\n" \
  "v_pk_mov_b32 v[62:63], 0, 0\n" \
  "buffer_load_dwordx4 v[212:215], v241, %[srd], %[so] offen offset:1568 sc1\n" \
  "v_pk_mov_b32 v[64:65], 0, 0\n" \
  "v_pk_mov_b32 v[66:67], 0, 0\n" \
  "v_pk_mov_b32 v[68:69], 0, 0\n" \
  "v_pk_mov_b32 v[70:71], 0, 0\n" \
  "v_pk_mov_b32 v[72:73], 0, 0\n" \
  "v_pk_mov_b32 v[74:75], 0, 0\n" \
  "v_pk_mov_b32 v[76:77], 0, 0\n" \
  "v_pk_mov_b32 v[78:79], 0, 0\n" \
  "s_mov_b32 %[so], 4224\n" \
  "buffer_load_dwordx4 v[216:219], v240, %[srd], %[so] offen sc1\n" \
  "v_pk_mov_b32 v[80:81], 0, 0\n" \
  "v_pk_mov_b32 v[82:83], 0, 0\n" \
  "v_pk_mov_b32 v[84:85], 0, 0\n" \
  "v_pk_mov_b32 v[86:87], 0, 0\n" \
  "v_pk_mov_b32 v[88:89], 0, 0\n" \
  "v_pk_mov_b32 v[90:91], 0, 0\n" \
  "v_pk_mov_b32 v[92:93], 0, 0\n" \
  "v_pk_mov_b32 v[94:95], 0, 0\n" \
  "buffer_load_dwordx4 v[220:223], v241, %[srd], %[so] offen offset:1024 sc1\n" \
  "v_pk_mov_b32 v[96:97], 0, 0\n" \
  "v_pk_mov_b32 v[98:99], 0, 0\n" \
  "v_pk_mov_b32 v[100:101], 0, 0\n" \
  "v_pk_mov_b32 v[102:103], 0, 0\n" \
  "v_pk_mov_b32 v[104:105], 0, 0\n" \
  "v_pk_mov_b32 v[106:107], 0, 0\n" \
  "v_pk_mov_b32 v[108:109], 0, 0\n" \
  "v_pk_mov_b32 v[110:111], 0, 0\n" \
  "buffer_load_dwordx4 v[224:227], v241, %[srd], %[so] offen offset:1568 sc1\n" \
  "v_pk_mov_b32 v[112:113], 0, 0\n" \
  "v_pk_mov_b32 v[114:115], 0, 0\n" \
  "v_pk_mov_b32 v[116:117], 0, 0\n" \
  "v_pk_mov_b32 v[118:119], 0, 0\n" \
  "v_pk_mov_b32 v[120:121], 0, 0\n" \
  "v_pk_mov_b32 v[122:123], 0, 0\n" \
  "v_pk_mov_b32 v[124:125], 0, 0\n" \
  "v_pk_mov_b32 v[126:127], 0, 0\n" \
  "v_mov_b32 v16, %[c6]\n" \
  "v_mov_b32 v17, 1\n" \
  "ds_add_u32 v16, v17\n" \
  "v_pk_mov_b32 v[128:129], 0, 0\n" \
  "v_pk_mov_b32 v[130:131], 0, 0\n" \
  "v_pk_mov_b32 v[132:133], 0, 0\n" \
  "v_pk_mov_b32 v[134:135], 0, 0\n" \
  "v_pk_mov_b32 v[136:137], 0, 0\n" \
  "v_pk_mov_b32 v[138:139], 0, 0\n" \
  "v_pk_mov_b32 v[140:141], 0, 0\n" \
  "v_pk_mov_b32 v[142:143], 0, 0\n" \
  "v_pk_mov_b32 v[144:145], 0, 0\n" \
  "v_pk_mov_b32 v[146:147], 0, 0\n" \
  "v_pk_mov_b32 v[148:149], 0, 0\n" \
  "v_pk_mov_b32 v[150:151], 0, 0\n" \
  "v_pk_mov_b32 v[152:153], 0, 0\n" \
  "v_pk_mov_b32 v[154:155], 0, 0\n" \
  "v_pk_mov_b32 v[156:157], 0, 0\n" \
  "v_pk_mov_b32 v[158:159], 0, 0\n" \
  "v_pk_mov_b32 v[24:25], 0, 0\n" \
  "v_pk_mov_b32 v[26:27], 0, 0\n" \
  "v_pk_mov_b32 v[28:29], 0, 0\n" \
  "v_pk_mov_b32 v[30:31], 0, 0\n" \
  "v_pk_mov_b32 v[236:237], 0, 0\n" \
  "v_pk_mov_b32 v[238:239], 0, 0\n" \
  "s_mov_b32 %[cnt], 4\n" \
  "s_waitcnt vmcnt(6)\n" \
  "v_mfma_f32_32x32x16_bf16 v[0:15], v[192:195], v[160:163], 0\n" \
  "s_nop 7\n" \
  "s_nop 3\n" \
  "LOOP%=:\n" \
  "v_exp_f32 v0, v0\n" \
  "v_exp_f32 v1, v1\n" \
  "v_smfmac_f32_32x32x32_bf16 v[144:159], v[236:239], v[24:31], v242\n" \
  "s_add_u32 %[so], %[so], 2112\n" \
  "v_exp_f32 v2, v2\n" \
  "v_exp_f32 v3, v3\n" \
  "v_exp_f32 v4, v4\n" \
  "v_mfma_f32_32x32x16_bf16 v[16:31], v[192:195], v[164:167], 0\n" \
  "v_exp_f32 v5, v5\n" \
  "v_exp_f32 v6, v6\n" \
  "v_exp_f32 v7, v7\n" \
  "v_exp_f32 v8, v8\n" \
  "v_exp_f32 v9, v9\n" \
  "v_smfmac_f32_32x32x32_bf16 v[32:47], v[196:199], v[0:7], v242\n" \
  "v_exp_f32 v10, v10\n" \
  "v_exp_f32 v11, v11\n" \
  "v_exp_f32 v12, v12\n" \
  "v_exp_f32 v13, v13\n" \
  "v_exp_f32 v14, v14\n" \
  "v_exp_f32 v15, v15\n" \
  "v_exp_f32 v16, v16\n" \
  "v_exp_f32 v17, v17\n" \
  "v_smfmac_f32_32x32x32_bf16 v[32:47], v[200:203], v[8:15], v242\n" \
  "buffer_load_dwordx4 v[228:231], v240, %[srd], %[so] offen sc1\n" \
  "v_exp_f32 v18, v18\n" \
  "v_exp_f32 v19, v19\n" \
  "v_exp_f32 v20, v20\n" \
  "v_mfma_f32_32x32x16_bf16 v[0:15], v[192:195], v[168:171], 0\n" \
  "v_exp_f32 v21, v21\n" \
  "v_exp_f32 v22, v22\n" \
  "v_exp_f32 v23, v23\n" \
  "v_exp_f32 v24, v24\n" \
  "v_exp_f32 v25, v25\n" \
  "v_smfmac_f32_32x32x32_bf16 v[48:63], v[196:199], v[16:23], v242\n" \
  "v_exp_f32 v26, v26\n" \
  "v_exp_f32 v27, v27\n" \
  "v_exp_f32 v28, v28\n" \
  "v_exp_f32 v29, v29\n" \
  "v_exp_f32 v30, v30\n" \
  "v_exp_f32 v31, v31\n" \
  "v_exp_f32 v0, v0\n" \
  "v_exp_f32 v1, v1\n" \
  "v_smfmac_f32_32x32x32_bf16 v[48:63], v[200:203], v[24:31], v242\n" \
  "v_exp_f32 v2, v2\n" \
  "v_exp_f32 v3, v3\n" \
  "v_exp_f32 v4, v4\n" \
  "v_mfma_f32_32x32x16_bf16 v[16:31], v[192:195], v[172:175], 0\n" \
  "v_exp_f32 v5, v5\n" \
  "v_exp_f32 v6, v6\n" \
  "v_exp_f32 v7, v7\n" \
  "v_exp_f32 v8, v8\n" \
  "v_exp_f32 v9, v9\n" \
  "v_smfmac_f32_32x32x32_bf16 v[64:79], v[196:199], v[0:7], v242\n" \
  "v_exp_f32 v10, v10\n" \
  "v_exp_f32 v11, v11\n" \
  "v_exp_f32 v12, v12\n" \
  "v_exp_f32 v13, v13\n" \
  "v_exp_f32 v14, v14\n" \
  "v_exp_f32 v15, v15\n" \
  "v_exp_f32 v16, v16\n" \
  "v_exp_f32 v17, v17\n" \
  "v_smfmac_f32_32x32x32_bf16 v[64:79], v[200:203], v[8:15], v242\n" \
  "buffer_load_dwordx4 v[232:235], v241, %[srd], %[so] offen offset:1024 sc1\n" \
  "v_exp_f32 v18, v18\n" \
  "v_exp_f32 v19, v19\n" \
  "v_exp_f32 v20, v20\n" \
  "v_mfma_f32_32x32x16_bf16 v[0:15], v[192:195], v[176:179], 0\n" \
  "v_exp_f32 v21, v21\n" \
  "v_exp_f32 v22, v22\n" \
  "v_exp_f32 v23, v23\n" \
  "v_exp_f32 v24, v24\n" \
  "v_exp_f32 v25, v25\n" \
  "v_smfmac_f32_32x32x32_bf16 v[80:95], v[196:199], v[16:23], v242\n" \
  "v_exp_f32 v26, v26\n" \
  "v_exp_f32 v27, v27\n" \
  "v_exp_f32 v28, v28\n" \
  "v_exp_f32 v29, v29\n" \
  "v_exp_f32 v30, v30\n" \
  "v_exp_f32 v31, v31\n" \
  "v_exp_f32 v0, v0\n" \
  "v_exp_f32 v1, v1\n" \
  "v_smfmac_f32_32x32x32_bf16 v[80:95], v[200:203], v[24:31], v242\n" \
  "v_exp_f32 v2, v2\n" \
  "v_exp_f32 v3, v3\n" \
  "v_exp_f32 v4, v4\n" \
  "v_mfma_f32_32x32x16_bf16 v[16:31], v[192:195], v[180:183], 0\n" \
  "v_exp_f32 v5, v5\n" \
  "v_exp_f32 v6, v6\n" \
  "v_exp_f32 v7, v7\n" \
  "v_exp_f32 v8, v8\n" \
  "v_exp_f32 v9, v9\n" \
  "v_smfmac_f32_32x32x32_bf16 v[96:111], v[196:199], v[0:7], v242\n" \
  "v_exp_f32 v10, v10\n" \
  "v_exp_f32 v11, v11\n" \
  "v_exp_f32 v12, v12\n" \
  "v_exp_f32 v13, v13\n" \
  "v_exp_f32 v14, v14\n" \
  "v_exp_f32 v15, v15\n" \
  "v_exp_f32 v16, v16\n" \
  "v_exp_f32 v17, v17\n" \
  "v_smfmac_f32_32x32x32_bf16 v[96:111], v[200:203], v[8:15], v242\n" \
  "buffer_load_dwordx4 v[236:239], v241, %[srd], %[so] offen offset:1568 sc1\n" \
  "v_exp_f32 v18, v18\n" \
  "v_exp_f32 v19, v19\n" \
  "v_exp_f32 v20, v20\n" \
  "v_mfma_f32_32x32x16_bf16 v[0:15], v[192:195], v[184:187], 0\n" \
  "v_exp_f32 v21, v21\n" \
  "v_exp_f32 v22, v22\n" \
  "v_exp_f32 v23, v23\n" \
  "v_exp_f32 v24, v24\n" \
  "v_exp_f32 v25, v25\n" \
  "v_smfmac_f32_32x32x32_bf16 v[112:127], v[196:199], v[16:23], v242\n" \
  "v_exp_f32 v26, v26\n" \
  "v_exp_f32 v27, v27\n" \
  "v_exp_f32 v28, v28\n" \
  "v_exp_f32 v29, v29\n" \
  "v_exp_f32 v30, v30\n" \
  "v_exp_f32 v31, v31\n" \
  "v_exp_f32 v0, v0\n" \
  "v_exp_f32 v1, v1\n" \
  "v_smfmac_f32_32x32x32_bf16 v[112:127], v[200:203], v[24:31], v242\n" \
  "v_exp_f32 v2, v2\n" \
  "v_exp_f32 v3, v3\n" \
  "v_exp_f32 v4, v4\n" \
  "v_mfma_f32_32x32x16_bf16 v[16:31], v[192:195], v[188:191], 0\n" \
  "v_exp_f32 v5, v5\n" \
  "v_exp_f32 v6, v6\n" \
  "v_exp_f32 v7, v7\n" \
  "v_exp_f32 v8, v8\n" \
  "v_exp_f32 v9, v9\n" \
  "v_smfmac_f32_32x32x32_bf16 v[128:143], v[196:199], v[0:7], v242\n" \
  "v_exp_f32 v10, v10\n" \
  "v_exp_f32 v11, v11\n" \
  "v_exp_f32 v12, v12\n" \
  "v_exp_f32 v13, v13\n" \
  "v_exp_f32 v14, v14\n" \
  "v_exp_f32 v15, v15\n" \
  "v_exp_f32 v16, v16\n" \
  "v_exp_f32 v17, v17\n" \
  "v_smfmac_f32_32x32x32_bf16 v[128:143], v[200:203], v[8:15], v242\n" \
  "s_waitcnt vmcnt(6)\n" \
  "v_exp_f32 v18, v18\n" \
  "v_exp_f32 v19, v19\n" \
  "v_exp_f32 v20, v20\n" \
  "v_mfma_f32_32x32x16_bf16 v[0:15], v[204:207], v[160:163], 0\n" \
  "v_exp_f32 v21, v21\n" \
  "v_exp_f32 v22, v22\n" \
  "v_exp_f32 v23, v23\n" \
  "v_exp_f32 v24, v24\n" \
  "v_exp_f32 v25, v25\n" \
  "v_smfmac_f32_32x32x32_bf16 v[144:159], v[196:199], v[16:23], v242\n" \
  "v_exp_f32 v26, v26\n" \
  "v_exp_f32 v27, v27\n" \
  "v_exp_f32 v28, v28\n" \
  "v_exp_f32 v29, v29\n" \
  "v_exp_f32 v30, v30\n" \
  "v_exp_f32 v31, v31\n" \
  "v_exp_f32 v0, v0\n" \
  "v_exp_f32 v1, v1\n" \
  "v_smfmac_f32_32x32x32_bf16 v[144:159], v[200:203], v[24:31], v242\n" \
  "s_add_u32 %[so], %[so], 2112\n" \
  "v_exp_f32 v2, v2\n" \
  "v_exp_f32 v3, v3\n" \
  "v_exp_f32 v4, v4\n" \
  "v_mfma_f32_32x32x16_bf16 v[16:31], v[204:207], v[164:167], 0\n" \
  "v_exp_f32 v5, v5\n" \
  "v_exp_f32 v6, v6\n" \
  "v_exp_f32 v7, v7\n" \
  "v_exp_f32 v8, v8\n" \
  "v_exp_f32 v9, v9\n" \
  "v_smfmac_f32_32x32x32_bf16 v[32:47], v[208:211], v[0:7], v242\n" \
  "v_exp_f32 v10, v10\n" \
  "v_exp_f32 v11, v11\n" \
  "v_exp_f32 v12, v12\n" \
  "v_exp_f32 v13, v13\n" \
  "v_exp_f32 v14, v14\n" \
  "v_exp_f32 v15, v15\n" \
  "v_exp_f32 v16, v16\n" \
  "v_exp_f32 v17, v17\n" \
  "v_smfmac_f32_32x32x32_bf16 v[32:47], v[212:215], v[8:15], v242\n" \
  "buffer_load_dwordx4 v[192:195], v240, %[srd], %[so] offen sc1\n" \
  "v_exp_f32 v18, v18\n" \
  "v_exp_f32 v19, v19\n" \
  "v_exp_f32 v20, v20\n" \
  "v_mfma_f32_32x32x16_bf16 v[0:15], v[204:207], v[168:171], 0\n" \
  "v_exp_f32 v21, v21\n" \
  "v_exp_f32 v22, v22\n" \
  "v_exp_f32 v23, v23\n" \
  "v_exp_f32 v24, v24\n" \
  "v_exp_f32 v25, v25\n" \
  "v_smfmac_f32_32x32x32_bf16 v[48:63], v[208:211], v[16:23], v242\n" \
  "v_exp_f32 v26, v26\n" \
  "v_exp_f32 v27, v27\n" \
  "v_exp_f32 v28, v28\n" \
  "v_exp_f32 v29, v29\n" \
  "v_exp_f32 v30, v30\n" \
  "v_exp_f32 v31, v31\n" \
  "v_exp_f32 v0, v0\n" \
  "v_exp_f32 v1, v1\n" \
  "v_smfmac_f32_32x32x32_bf16 v[48:63], v[212:215], v[24:31], v242\n" \
  "v_exp_f32 v2, v2\n" \
  "v_exp_f32 v3, v3\n" \
  "v_exp_f32 v4, v4\n" \
  "v_mfma_f32_32x32x16_bf16 v[16:31], v[204:207], v[172:175], 0\n" \
  "v_exp_f32 v5, v5\n" \
  "v_exp_f32 v6, v6\n" \
  "v_exp_f32 v7, v7\n" \
  "v_exp_f32 v8, v8\n" \
  "v_exp_f32 v9, v9\n" \
  "v_smfmac_f32_32x32x32_bf16 v[64:79], v[208:211], v[0:7], v242\n" \
  "v_exp_f32 v10, v10\n" \
  "v_exp_f32 v11, v11\n" \
  "v_exp_f32 v12, v12\n" \
  "v_exp_f32 v13, v13\n" \
  "v_exp_f32 v14, v14\n" \
  "v_exp_f32 v15, v15\n" \
  "v_exp_f32 v16, v16\n" \
  "v_exp_f32 v17, v17\n" \
  "v_smfmac_f32_32x32x32_bf16 v[64:79], v[212:215], v[8:15], v242\n" \
  "buffer_load_dwordx4 v[196:199], v241, %[srd], %[so] offen offset:1024 sc1\n" \
  "v_exp_f32 v18, v18\n" \
  "v_exp_f32 v19, v19\n" \
  "v_exp_f32 v20, v20\n" \
  "v_mfma_f32_32x32x16_bf16 v[0:15], v[204:207], v[176:179], 0\n" \
  "v_exp_f32 v21, v21\n" \
  "v_exp_f32 v22, v22\n" \
  "v_exp_f32 v23, v23\n" \
  "v_exp_f32 v24, v24\n" \
  "v_exp_f32 v25, v25\n" \
  "v_smfmac_f32_32x32x32_bf16 v[80:95], v[208:211], v[16:23], v242\n" \
  "v_exp_f32 v26, v26\n" \
  "v_exp_f32 v27, v27\n" \
  "v_exp_f32 v28, v28\n" \
  "v_exp_f32 v29, v29\n" \
  "v_exp_f32 v30, v30\n" \
  "v_exp_f32 v31, v31\n" \
  "v_exp_f32 v0, v0\n" \
  "v_exp_f32 v1, v1\n" \
  "v_smfmac_f32_32x32x32_bf16 v[80:95], v[212:215], v[24:31], v242\n" \
  "v_exp_f32 v2, v2\n" \
  "v_exp_f32 v3, v3\n" \
  "v_exp_f32 v4, v4\n" \
  "v_mfma_f32_32x32x16_bf16 v[16:31], v[204:207], v[180:183], 0\n" \
  "v_exp_f32 v5, v5\n" \
  "v_exp_f32 v6, v6\n" \
  "v_exp_f32 v7, v7\n" \
  "v_exp_f32 v8, v8\n" \
  "v_exp_f32 v9, v9\n" \
  "v_smfmac_f32_32x32x32_bf16 v[96:111], v[208:211], v[0:7], v242\n" \
  "v_exp_f32 v10, v10\n" \
  "v_exp_f32 v11, v11\n" \
  "v_exp_f32 v12, v12\n" \
  "v_exp_f32 v13, v13\n" \
  "v_exp_f32 v14, v14\n" \
  "v_exp_f32 v15, v15\n" \
  "v_exp_f32 v16, v16\n" \
  "v_exp_f32 v17, v17\n" \
  "v_smfmac_f32_32x32x32_bf16 v[96:111], v[212:215], v[8:15], v242\n" \
  "buffer_load_dwordx4 v[200:203], v241, %[srd], %[so] offen offset:1568 sc1\n" \
  "v_exp_f32 v18, v18\n" \
  "v_exp_f32 v19, v19\n" \
  "v_exp_f32 v20, v20\n" \
  "v_mfma_f32_32x32x16_bf16 v[0:15], v[204:207], v[184:187], 0\n" \
  "v_exp_f32 v21, v21\n" \
  "v_exp_f32 v22, v22\n" \
  "v_exp_f32 v23, v23\n" \
  "v_exp_f32 v24, v24\n" \
  "v_exp_f32 v25, v25\n" \
  "v_smfmac_f32_32x32x32_bf16 v[112:127], v[208:211], v[16:23], v242\n" \
  "v_exp_f32 v26, v26\n" \
  "v_exp_f32 v27, v27\n" \
  "v_exp_f32 v28, v28\n" \
  "v_exp_f32 v29, v29\n" \
  "v_exp_f32 v30, v30\n" \
  "v_exp_f32 v31, v31\n" \
  "v_exp_f32 v0, v0\n" \
  "v_exp_f32 v1, v1\n" \
  "v_smfmac_f32_32x32x32_bf16 v[112:127], v[212:215], v[24:31], v242\n" \
  "v_exp_f32 v2, v2\n" \
  "v_exp_f32 v3, v3\n" \
  "v_exp_f32 v4, v4\n" \
  "v_mfma_f32_32x32x16_bf16 v[16:31], v[204:207], v[188:191], 0\n" \
  "v_exp_f32 v5, v5\n" \
  "v_exp_f32 v6, v6\n" \
  "v_exp_f32 v7, v7\n" \
  "v_exp_f32 v8, v8\n" \
  "v_exp_f32 v9, v9\n" \
  "v_smfmac_f32_32x32x32_bf16 v[128:143], v[208:211], v[0:7], v242\n" \
  "v_exp_f32 v10, v10\n" \
  "v_exp_f32 v11, v11\n" \
  "v_exp_f32 v12, v12\n" \
  "v_exp_f32 v13, v13\n" \
  "v_exp_f32 v14, v14\n" \
  "v_exp_f32 v15, v15\n" \
  "v_exp_f32 v16, v16\n" \
  "v_exp_f32 v17, v17\n" \
  "v_smfmac_f32_32x32x32_bf16 v[128:143], v[212:215], v[8:15], v242\n" \
  "s_waitcnt vmcnt(6)\n" \
  "v_exp_f32 v18, v18\n" \
  "v_exp_f32 v19, v19\n" \
  "v_exp_f32 v20, v20\n" \
  "v_mfma_f32_32x32x16_bf16 v[0:15], v[216:219], v[160:163], 0\n" \
  "v_exp_f32 v21, v21\n" \
  "v_exp_f32 v22, v22\n" \
  "v_exp_f32 v23, v23\n" \
  "v_exp_f32 v24, v24\n" \
  "v_exp_f32 v25, v25\n" \
  "v_smfmac_f32_32x32x32_bf16 v[144:159], v[208:211], v[16:23], v242\n" \
  "v_exp_f32 v26, v26\n" \
  "v_exp_f32 v27, v27\n" \
  "v_exp_f32 v28, v28\n" \
  "v_exp_f32 v29, v29\n" \
  "v_exp_f32 v30, v30\n" \
  "v_exp_f32 v31, v31\n" \
  "v_exp_f32 v0, v0\n" \
  "v_exp_f32 v1, v1\n" \
  "v_smfmac_f32_32x32x32_bf16 v[144:159], v[212:215], v[24:31], v242\n" \
  "s_add_u32 %[so], %[so], 2112\n" \
  "v_exp_f32 v2, v2\n" \
  "v_exp_f32 v3, v3\n" \
  "v_exp_f32 v4, v4\n" \
  "v_mfma_f32_32x32x16_bf16 v[16:31], v[216:219], v[164:167], 0\n" \
  "v_exp_f32 v5, v5\n" \
  "v_exp_f32 v6, v6\n" \
  "v_exp_f32 v7, v7\n" \
  "v_exp_f32 v8, v8\n" \
  "v_exp_f32 v9, v9\n" \
  "v_smfmac_f32_32x32x32_bf16 v[32:47], v[220:223], v[0:7], v242\n" \
  "v_exp_f32 v10, v10\n" \
  "v_exp_f32 v11, v11\n" \
  "v_exp_f32 v12, v12\n" \
  "v_exp_f32 v13, v13\n" \
  "v_exp_f32 v14, v14\n" \
  "v_exp_f32 v15, v15\n" \
  "v_exp_f32 v16, v16\n" \
  "v_exp_f32 v17, v17\n" \
  "v_smfmac_f32_32x32x32_bf16 v[32:47], v[224:227], v[8:15], v242\n" \
  "buffer_load_dwordx4 v[204:207], v240, %[srd], %[so] offen sc1\n" \
  "v_exp_f32 v18, v18\n" \
  "v_exp_f32 v19, v19\n" \
  "v_exp_f32 v20, v20\n" \
  "v_mfma_f32_32x32x16_bf16 v[0:15], v[216:219], v[168:171], 0\n" \
  "v_exp_f32 v21, v21\n" \
  "v_exp_f32 v22, v22\n" \
  "v_exp_f32 v23, v23\n" \
  "v_exp_f32 v24, v24\n" \
  "v_exp_f32 v25, v25\n" \
  "v_smfmac_f32_32x32x32_bf16 v[48:63], v[220:223], v[16:23], v242\n" \
  "v_exp_f32 v26, v26\n" \
  "v_exp_f32 v27, v27\n" \
  "v_exp_f32 v28, v28\n" \
  "v_exp_f32 v29, v29\n" \
  "v_exp_f32 v30, v30\n" \
  "v_exp_f32 v31, v31\n" \
  "v_exp_f32 v0, v0\n" \
  "v_exp_f32 v1, v1\n" \
  "v_smfmac_f32_32x32x32_bf16 v[48:63], v[224:227], v[24:31], v242\n" \
  "v_exp_f32 v2, v2\n" \
  "v_exp_f32 v3, v3\n" \
  "v_exp_f32 v4, v4\n" \
  "v_mfma_f32_32x32x16_bf16 v[16:31], v[216:219], v[172:175], 0\n" \
  "v_exp_f32 v5, v5\n" \
  "v_exp_f32 v6, v6\n" \
  "v_exp_f32 v7, v7\n" \
  "v_exp_f32 v8, v8\n" \
  "v_exp_f32 v9, v9\n" \
  "v_smfmac_f32_32x32x32_bf16 v[64:79], v[220:223], v[0:7], v242\n" \
  "v_exp_f32 v10, v10\n" \
  "v_exp_f32 v11, v11\n" \
  "v_exp_f32 v12, v12\n" \
  "v_exp_f32 v13, v13\n" \
  "v_exp_f32 v14, v14\n" \
  "v_exp_f32 v15, v15\n" \
  "v_exp_f32 v16, v16\n" \
  "v_exp_f32 v17, v17\n" \
  "v_smfmac_f32_32x32x32_bf16 v[64:79], v[224:227], v[8:15], v242\n" \
  "buffer_load_dwordx4 v[208:211], v241, %[srd], %[so] offen offset:1024 sc1\n" \
  "v_exp_f32 v18, v18\n" \
  "v_exp_f32 v19, v19\n" \
  "v_exp_f32 v20, v20\n" \
  "v_mfma_f32_32x32x16_bf16 v[0:15], v[216:219], v[176:179], 0\n" \
  "v_exp_f32 v21, v21\n" \
  "v_exp_f32 v22, v22\n" \
  "v_exp_f32 v23, v23\n" \
  "v_exp_f32 v24, v24\n" \
  "v_exp_f32 v25, v25\n" \
  "v_smfmac_f32_32x32x32_bf16 v[80:95], v[220:223], v[16:23], v242\n" \
  "v_exp_f32 v26, v26\n" \
  "v_exp_f32 v27, v27\n" \
  "v_exp_f32 v28, v28\n" \
  "v_exp_f32 v29, v29\n" \
  "v_exp_f32 v30, v30\n" \
  "v_exp_f32 v31, v31\n" \
  "v_exp_f32 v0, v0\n" \
  "v_exp_f32 v1, v1\n" \
  "v_smfmac_f32_32x32x32_bf16 v[80:95], v[224:227], v[24:31], v242\n" \
  "v_exp_f32 v2, v2\n" \
  "v_exp_f32 v3, v3\n" \
  "v_exp_f32 v4, v4\n" \
  "v_mfma_f32_32x32x16_bf16 v[16:31], v[216:219], v[180:183], 0\n" \
  "v_exp_f32 v5, v5\n" \
  "v_exp_f32 v6, v6\n" \
  "v_exp_f32 v7, v7\n" \
  "v_exp_f32 v8, v8\n" \
  "v_exp_f32 v9, v9\n" \
  "v_smfmac_f32_32x32x32_bf16 v[96:111], v[220:223], v[0:7], v242\n" \
  "v_exp_f32 v10, v10\n" \
  "v_exp_f32 v11, v11\n" \
  "v_exp_f32 v12, v12\n" \
  "v_exp_f32 v13, v13\n" \
  "v_exp_f32 v14, v14\n" \
  "v_exp_f32 v15, v15\n" \
  "v_exp_f32 v16, v16\n" \
  "v_exp_f32 v17, v17\n" \
  "v_smfmac_f32_32x32x32_bf16 v[96:111], v[224:227], v[8:15], v242\n" \
  "buffer_load_dwordx4 v[212:215], v241, %[srd], %[so] offen offset:1568 sc1\n" \
  "v_exp_f32 v18, v18\n" \
  "v_exp_f32 v19, v19\n" \
  "v_exp_f32 v20, v20\n" \
  "v_mfma_f32_32x32x16_bf16 v[0:15], v[216:219], v[184:187], 0\n" \
  "v_exp_f32 v21, v21\n" \
  "v_exp_f32 v22, v22\n" \
  "v_exp_f32 v23, v23\n" \
  "v_exp_f32 v24, v24\n" \
  "v_exp_f32 v25, v25\n" \
  "v_smfmac_f32_32x32x32_bf16 v[112:127], v[220:223], v[16:23], v242\n" \
  "v_exp_f32 v26, v26\n" \
  "v_exp_f32 v27, v27\n" \
  "v_exp_f32 v28, v28\n" \
  "v_exp_f32 v29, v29\n" \
  "v_exp_f32 v30, v30\n" \
  "v_exp_f32 v31, v31\n" \
  "v_exp_f32 v0, v0\n" \
  "v_exp_f32 v1, v1\n" \
  "v_smfmac_f32_32x32x32_bf16 v[112:127], v[224:227], v[24:31], v242\n" \
  "v_exp_f32 v2, v2\n" \
  "v_exp_f32 v3, v3\n" \
  "v_exp_f32 v4, v4\n" \
  "v_mfma_f32_32x32x16_bf16 v[16:31], v[216:219], v[188:191], 0\n" \
  "v_exp_f32 v5, v5\n" \
  "v_exp_f32 v6, v6\n" \
  "v_exp_f32 v7, v7\n" \
  "v_exp_f32 v8, v8\n" \
  "v_exp_f32 v9, v9\n" \
  "v_smfmac_f32_32x32x32_bf16 v[128:143], v[220:223], v[0:7], v242\n" \
  "v_exp_f32 v10, v10\n" \
  "v_exp_f32 v11, v11\n" \
  "v_exp_f32 v12, v12\n" \
  "v_exp_f32 v13, v13\n" \
  "v_exp_f32 v14, v14\n" \
  "v_exp_f32 v15, v15\n" \
  "v_exp_f32 v16, v16\n" \
  "v_exp_f32 v17, v17\n" \
  "v_smfmac_f32_32x32x32_bf16 v[128:143], v[224:227], v[8:15], v242\n" \
  "s_waitcnt vmcnt(6)\n" \
  "v_exp_f32 v18, v18\n" \
  "v_exp_f32 v19, v19\n" \
  "v_exp_f32 v20, v20\n" \
  "v_mfma_f32_32x32x16_bf16 v[0:15], v[228:231], v[160:163], 0\n" \
  "v_exp_f32 v21, v21\n" \
  "v_exp_f32 v22, v22\n" \
  "v_exp_f32 v23, v23\n" \
  "v_exp_f32 v24, v24\n" \
  "v_exp_f32 v25, v25\n" \
  "v_smfmac_f32_32x32x32_bf16 v[144:159], v[220:223], v[16:23], v242\n" \
  "v_exp_f32 v26, v26\n" \
  "v_exp_f32 v27, v27\n" \
  "v_exp_f32 v28, v28\n" \
  "v_exp_f32 v29, v29\n" \
  "v_exp_f32 v30, v30\n" \
  "v_exp_f32 v31, v31\n" \
  "v_exp_f32 v0, v0\n" \
  "v_exp_f32 v1, v1\n" \
  "v_smfmac_f32_32x32x32_bf16 v[144:159], v[224:227], v[24:31], v242\n" \
  "s_add_u32 %[so], %[so], 2112\n" \
  "v_exp_f32 v2, v2\n" \
  "v_exp_f32 v3, v3\n" \
  "v_exp_f32 v4, v4\n" \
  "v_mfma_f32_32x32x16_bf16 v[16:31], v[228:231], v[164:167], 0\n" \
  "v_exp_f32 v5, v5\n" \
  "v_exp_f32 v6, v6\n" \
  "v_exp_f32 v7, v7\n" \
  "v_exp_f32 v8, v8\n" \
  "v_exp_f32 v9, v9\n" \
  "v_smfmac_f32_32x32x32_bf16 v[32:47], v[232:235], v[0:7], v242\n" \
  "v_exp_f32 v10, v10\n" \
  "v_exp_f32 v11, v11\n" \
  "v_exp_f32 v12, v12\n" \
  "v_exp_f32 v13, v13\n" \
  "v_exp_f32 v14, v14\n" \
  "v_exp_f32 v15, v15\n" \
  "v_exp_f32 v16, v16\n" \
  "v_exp_f32 v17, v17\n" \
  "v_smfmac_f32_32x32x32_bf16 v[32:47], v[236:239], v[8:15], v242\n" \
  "buffer_load_dwordx4 v[216:219], v240, %[srd], %[so] offen sc1\n" \
  "v_exp_f32 v18, v18\n" \
  "v_exp_f32 v19, v19\n" \
  "v_exp_f32 v20, v20\n" \
  "v_mfma_f32_32x32x16_bf16 v[0:15], v[228:231], v[168:171], 0\n" \
  "v_exp_f32 v21, v21\n" \
  "v_exp_f32 v22, v22\n" \
  "v_exp_f32 v23, v23\n" \
  "v_exp_f32 v24, v24\n" \
  "v_exp_f32 v25, v25\n" \
  "v_smfmac_f32_32x32x32_bf16 v[48:63], v[232:235], v[16:23], v242\n" \
  "v_exp_f32 v26, v26\n" \
  "v_exp_f32 v27, v27\n" \
  "v_exp_f32 v28, v28\n" \
  "v_exp_f32 v29, v29\n" \
  "v_exp_f32 v30, v30\n" \
  "v_exp_f32 v31, v31\n" \
  "v_exp_f32 v0, v0\n" \
  "v_exp_f32 v1, v1\n" \
  "v_smfmac_f32_32x32x32_bf16 v[48:63], v[236:239], v[24:31], v242\n" \
  "v_exp_f32 v2, v2\n" \
  "v_exp_f32 v3, v3\n" \
  "v_exp_f32 v4, v4\n" \
  "v_mfma_f32_32x32x16_bf16 v[16:31], v[228:231], v[172:175], 0\n" \
  "v_exp_f32 v5, v5\n" \
  "v_exp_f32 v6, v6\n" \
  "v_exp_f32 v7, v7\n" \
  "v_exp_f32 v8, v8\n" \
  "v_exp_f32 v9, v9\n" \
  "v_smfmac_f32_32x32x32_bf16 v[64:79], v[232:235], v[0:7], v242\n" \
  "v_exp_f32 v10, v10\n" \
  "v_exp_f32 v11, v11\n" \
  "v_exp_f32 v12, v12\n" \
  "v_exp_f32 v13, v13\n" \
  "v_exp_f32 v14, v14\n" \
  "v_exp_f32 v15, v15\n" \
  "v_exp_f32 v16, v16\n" \
  "v_exp_f32 v17, v17\n" \
  "v_smfmac_f32_32x32x32_bf16 v[64:79], v[236:239], v[8:15], v242\n" \
  "buffer_load_dwordx4 v[220:223], v241, %[srd], %[so] offen offset:1024 sc1\n" \
  "v_exp_f32 v18, v18\n" \
  "v_exp_f32 v19, v19\n" \
  "v_exp_f32 v20, v20\n" \
  "v_mfma_f32_32x32x16_bf16 v[0:15], v[228:231], v[176:179], 0\n" \
  "v_exp_f32 v21, v21\n" \
  "v_exp_f32 v22, v22\n" \
  "v_exp_f32 v23, v23\n" \
  "v_exp_f32 v24, v24\n" \
  "v_exp_f32 v25, v25\n" \
  "v_smfmac_f32_32x32x32_bf16 v[80:95], v[232:235], v[16:23], v242\n" \
  "v_exp_f32 v26, v26\n" \
  "v_exp_f32 v27, v27\n" \
  "v_exp_f32 v28, v28\n" \
  "v_exp_f32 v29, v29\n" \
  "v_exp_f32 v30, v30\n" \
  "v_exp_f32 v31, v31\n" \
  "v_exp_f32 v0, v0\n" \
  "v_exp_f32 v1, v1\n" \
  "v_smfmac_f32_32x32x32_bf16 v[80:95], v[236:239], v[24:31], v242\n" \
  "v_exp_f32 v2, v2\n" \
  "v_exp_f32 v3, v3\n" \
  "v_exp_f32 v4, v4\n" \
  "v_mfma_f32_32x32x16_bf16 v[16:31], v[228:231], v[180:183], 0\n" \
  "v_exp_f32 v5, v5\n" \
  "v_exp_f32 v6, v6\n" \
  "v_exp_f32 v7, v7\n" \
  "v_exp_f32 v8, v8\n" \
  "v_exp_f32 v9, v9\n" \
  "v_smfmac_f32_32x32x32_bf16 v[96:111], v[232:235], v[0:7], v242\n" \
  "v_exp_f32 v10, v10\n" \
  "v_exp_f32 v11, v11\n" \
  "v_exp_f32 v12, v12\n" \
  "v_exp_f32 v13, v13\n" \
  "v_exp_f32 v14, v14\n" \
  "v_exp_f32 v15, v15\n" \
  "v_exp_f32 v16, v16\n" \
  "v_exp_f32 v17, v17\n" \
  "v_smfmac_f32_32x32x32_bf16 v[96:111], v[236:239], v[8:15], v242\n" \
  "buffer_load_dwordx4 v[224:227], v241, %[srd], %[so] offen offset:1568 sc1\n" \
  "v_exp_f32 v18, v18\n" \
  "v_exp_f32 v19, v19\n" \
  "v_exp_f32 v20, v20\n" \
  "v_mfma_f32_32x32x16_bf16 v[0:15], v[228:231], v[184:187], 0\n" \
  "v_exp_f32 v21, v21\n" \
  "v_exp_f32 v22, v22\n" \
  "v_exp_f32 v23, v23\n" \
  "v_exp_f32 v24, v24\n" \
  "v_exp_f32 v25, v25\n" \
  "v_smfmac_f32_32x32x32_bf16 v[112:127], v[232:235], v[16:23], v242\n" \
  "v_exp_f32 v26, v26\n" \
  "v_exp_f32 v27, v27\n" \
  "v_exp_f32 v28, v28\n" \
  "v_exp_f32 v29, v29\n" \
  "v_exp_f32 v30, v30\n" \
  "v_exp_f32 v31, v31\n" \
  "v_exp_f32 v0, v0\n" \
  "v_exp_f32 v1, v1\n" \
  "v_smfmac_f32_32x32x32_bf16 v[112:127], v[236:239], v[24:31], v242\n" \
  "v_exp_f32 v2, v2\n" \
  "v_exp_f32 v3, v3\n" \
  "v_exp_f32 v4, v4\n" \
  "v_mfma_f32_32x32x16_bf16 v[16:31], v[228:231], v[188:191], 0\n" \
  "v_exp_f32 v5, v5\n" \
  "v_exp_f32 v6, v6\n" \
  "v_exp_f32 v7, v7\n" \
  "v_exp_f32 v8, v8\n" \
  "v_exp_f32 v9, v9\n" \
  "v_smfmac_f32_32x32x32_bf16 v[128:143], v[232:235], v[0:7], v242\n" \
  "v_exp_f32 v10, v10\n" \
  "v_exp_f32 v11, v11\n" \
  "v_exp_f32 v12, v12\n" \
  "v_exp_f32 v13, v13\n" \
  "v_exp_f32 v14, v14\n" \
  "v_exp_f32 v15, v15\n" \
  "v_exp_f32 v16, v16\n" \
  "v_exp_f32 v17, v17\n" \
  "v_smfmac_f32_32x32x32_bf16 v[128:143], v[236:239], v[8:15], v242\n" \
  "s_waitcnt vmcnt(6)\n" \
  "s_sub_u32 %[cnt], %[cnt], 1\n" \
  "v_exp_f32 v18, v18\n" \
  "v_exp_f32 v19, v19\n" \
  "v_exp_f32 v20, v20\n" \
  "v_mfma_f32_32x32x16_bf16 v[0:15], v[192:195], v[160:163], 0\n" \
  "v_exp_f32 v21, v21\n" \
  "v_exp_f32 v22, v22\n" \
  "v_exp_f32 v23, v23\n" \
  "v_exp_f32 v24, v24\n" \
  "v_exp_f32 v25, v25\n" \
  "v_smfmac_f32_32x32x32_bf16 v[144:159], v[232:235], v[16:23], v242\n" \
  "s_cmp_lg_u32 %[cnt], 0\n" \
  "v_exp_f32 v26, v26\n" \
  "v_exp_f32 v27, v27\n" \
  "v_exp_f32 v28, v28\n" \
  "v_exp_f32 v29, v29\n" \
  "v_exp_f32 v30, v30\n" \
  "v_exp_f32 v31, v31\n" \
  "s_cbranch_scc1 LOOP%=\n" \
  "s_nop 1\n" \
  "v_smfmac_f32_32x32x32_bf16 v[144:159], v[236:239], v[24:31], v242\n" \
  "s_waitcnt vmcnt(0)\n" \
  "s_nop 7\n" \
  "s_nop 7\n" \
  ""
#define ATTN_CLOB "v0","v1","v2","v3","v4","v5","v6","v7","v8","v9","v10","v11","v12","v13","v14","v15","v16","v17","v18","v19","v20","v21","v22","v23","v24","v25","v26","v27","v28","v29","v30","v31","v204","v205","v206","v207","v208","v209","v210","v211","v212","v213","v214","v215","v216","v217","v218","v219","v220","v221","v222","v223","v224","v225","v226","v227","v228","v229","v230","v231","v232","v233","v234","v235","v236","v237","v238","v239"

constexpr int TS = 2112;
constexpr int WS_EPOCH = 0;
constexpr int WS_FKV = 32768;
constexpr int WS_FH = 229376;
constexpr int WS_KV = 65536;
constexpr int WS_E = 262144;
constexpr int WS_H = 524288, HS = 131072 + 128;
constexpr int WS_O = WS_H + 32 * HS, OSZ = 65536 + 128;
constexpr int WS_PROF = WS_O + 32 * OSZ;
constexpr int WS_END = WS_PROF + 262144 + 524288;
constexpr int L_XB = 0, L_WQ = 66560, L_WKV = 133120, L_OP = 16640;
constexpr int L_RING = 66560, L_TR = 133120, L_QX = 151552, L_KP = 151552, L_CNT = 163776;
constexpr int L_R2 = 65536, L_OX = 100352, L_TB = 133120;
constexpr unsigned POISON = 0xFFFFFFFFu;
#define PS8 2
#define PS16 1
constexpr unsigned OOB = 0x80000000u;
constexpr unsigned SPIN_MAX = 1u << 17;

DI rsrc_t mk(const void* p, int bytes) { return __builtin_amdgcn_make_buffer_rsrc((void*)p, (short)0, bytes, 0x00020000); }
DI void dma(rsrc_t r, unsigned voff, unsigned soff, unsigned lds) {
  asm volatile("s_mov_b32 m0, %0\n\ts_nop 0\n\tbuffer_load_dwordx4 %1, %2, %3 offen lds" :: "s"(lds), "v"(voff), "s"(r), "s"(soff) : "m0", "memory");
}
template <int DG, int DL> DI void dma2(rsrc_t r, unsigned voff, unsigned gbase, unsigned lbase) {
  unsigned t;
  asm volatile("s_add_u32 m0, %3, %5\n\ts_add_u32 %0, %2, %6\n\tbuffer_load_dwordx4 %1, %4, %0 offen lds"
               : "=&s"(t) : "v"(voff), "s"(gbase), "s"(lbase), "s"(r), "n"(DL), "n"(DG) : "m0", "scc", "memory");
}
DI void touch(rsrc_t r, unsigned voff, unsigned lds) {
  asm volatile("s_mov_b32 m0, %0\n\ts_nop 0\n\tbuffer_load_dword %1, %2, 0 offen sc1 lds" :: "s"(lds), "v"(voff), "s"(r) : "m0", "memory");
}
#define WM 0
#define CP 7
#define DS 0
#define FT 0
#define TLB 0
template <int I> struct IC { static constexpr int v = I; };
template <int N, int I = 0, class F> DI void sfor(F&& f) { if constexpr (I < N) { f(IC<I>{}); sfor<N, I + 1>(f); } }
#define TH 6
#define PACE 1
#define X1 3
#define HD 1
#define OD 1
#define WN 8
#define X2 2
#define X3 X2
#define XK 16
#define PR 11
#define WT 0
#define SL 0
#define ST 1
#define KA 1
#define AD 7
template <int N> DI void vmwait() { asm volatile("s_waitcnt vmcnt(%0)" :: "n"(N) : "memory"); }
DI void hbar() { asm volatile("s_waitcnt lgkmcnt(0)\n\ts_barrier" ::: "memory"); }
DI unsigned cvtpk(float lo, float hi) { return __builtin_bit_cast(unsigned, __builtin_convertvector(f32x2{lo, hi}, bf16x2)); }
DI float bf2f(unsigned short v) { return __uint_as_float((unsigned)v << 16); }
DI u32x4 lds128(unsigned a) { return *(const LDSP u32x4*)(uintptr_t)a; }
DI u32x2 ldstr(unsigned a) { return __builtin_bit_cast(u32x2, __builtin_amdgcn_ds_read_tr16_b64_v4i16((LDSP s16x4*)(uintptr_t)a)); }
DI u32x4 ldstr2(unsigned a0, unsigned a1) { u32x2 x = ldstr(a0), y = ldstr(a1); return u32x4{x[0], x[1], y[0], y[1]}; }
DI void sts128(unsigned a, u32x4 v) { *(LDSP u32x4*)(uintptr_t)a = v; }
DI void sts64(unsigned a, u32x2 v) { *(LDSP u32x2*)(uintptr_t)a = v; }
DI void sts32(unsigned a, unsigned v) { *(LDSP unsigned*)(uintptr_t)a = v; }
DI bf16x8 asbf(u32x4 v) { return __builtin_bit_cast(bf16x8, v); }
DI f32x16 mfma32(u32x4 a, u32x4 b, f32x16 c) { return __builtin_amdgcn_mfma_f32_32x32x16_bf16(asbf(a), asbf(b), c, 0, 0, 0); }
DI f32x4 mfma16(u32x4 a, u32x4 b, f32x4 c) { return __builtin_amdgcn_mfma_f32_16x16x32_bf16(asbf(a), asbf(b), c, 0, 0, 0); }
DI f32x4 asf4(u32x4 v) { return __builtin_bit_cast(f32x4, v); }
DI u32x4 asu4(f32x4 v) { return __builtin_bit_cast(u32x4, v); }

DI void poll8(rsrc_t ws, unsigned voff, unsigned e0, unsigned e1) {
#pragma unroll 1
  for (unsigned s = 0; s < SPIN_MAX; ++s) {
    u32x2 f;
    asm volatile("buffer_load_dwordx2 %0, %1, %2, 0 offen sc1\n\ts_waitcnt vmcnt(0)" : "=v"(f) : "v"(voff), "s"(ws) : "memory");
    bool ok = (voff == OOB) || (f[0] == e0 && f[1] == e1);
    if (__builtin_amdgcn_ballot_w64(!ok) == 0) break;
    __builtin_amdgcn_s_sleep(PS8);
  }
}
DI void poll32(rsrc_t ws, unsigned voff, unsigned e0, unsigned e1) {
#pragma unroll 1
  for (unsigned s = 0; s < SPIN_MAX; ++s) {
    u32x4 f, k;
    asm volatile("buffer_load_dwordx4 %0, %2, %3, 0 offen sc1\n\tbuffer_load_dwordx4 %1, %2, %3, 0 offen offset:1024 sc1\n\ts_waitcnt vmcnt(0)" : "=&v"(f), "=&v"(k) : "v"(voff), "s"(ws) : "memory");
    bool ok = (f[0] == e0 && f[1] == e1 && f[2] == e0 && f[3] == e1 && k[0] == e0 && k[1] == e1 && k[2] == e0 && k[3] == e1);
    if (__builtin_amdgcn_ballot_w64(!ok) == 0) break;
    __builtin_amdgcn_s_sleep(PS16);
  }
}
DI void cnt_add(unsigned addr, int lane) {
  asm volatile("s_waitcnt lgkmcnt(0)" ::: "memory");
  if (lane == 0) asm volatile("ds_add_u32 %0, %1" :: "v"(addr), "v"(1u) : "memory");
}
DI unsigned cnt_inc(unsigned addr, int lane) {
  unsigned v = 0;
  asm volatile("s_waitcnt lgkmcnt(0)" ::: "memory");
  if (lane == 0) asm volatile("ds_add_rtn_u32 %0, %1, %2\n\ts_waitcnt lgkmcnt(0)" : "=v"(v) : "v"(addr), "v"(1u) : "memory");
  return __builtin_amdgcn_readfirstlane(v);
}
template <int SLEEP> DI void cnt_wait(unsigned addr, unsigned target) {
#pragma unroll 1
  for (unsigned s = 0; s < 64 * SPIN_MAX; ++s) {
    unsigned v;
    asm volatile("ds_read_b32 %0, %1\n\ts_waitcnt lgkmcnt(0)" : "=v"(v) : "v"(addr) : "memory");
    if (__builtin_amdgcn_readfirstlane(v) >= target) break;
    if (SLEEP) __builtin_amdgcn_s_sleep(SLEEP);
  }
}
DI void sbar(unsigned addr, unsigned target, int lane) { cnt_add(addr, lane); cnt_wait<1>(addr, target); }
DI void sb0() { __builtin_amdgcn_sched_barrier(0); }
template <int X> DI void mpair(f32x16& c0, u32x4 a0, u32x4 b0, f32x16& c1, u32x4 a1, u32x4 b1) {
  if (X & 2) __builtin_amdgcn_s_setprio(0);
  c0 = mfma32(a0, b0, c0);
  if (X & 1) { sb0(); __builtin_amdgcn_s_sleep(1); sb0(); }
  c1 = mfma32(a1, b1, c1);
  if (X & 2) { sb0(); __builtin_amdgcn_s_setprio(3); }
}
DI f32x2 rot90(f32x2 a) { return f32x2{-a[1], a[0]}; }
#define SF 1
DI float sfma(float a, float b, float c) { float r; asm("v_fma_f32 %0, %1, %2, %3" : "=v"(r) : "v"(a), "v"(b), "v"(c)); return r; }
DI float smul(float a, float b) { float r; asm("v_mul_f32 %0, %1, %2" : "=v"(r) : "v"(a), "v"(b)); return r; }
DI f32x2 pfma(f32x2 a, f32x2 b, f32x2 c) { return f32x2{sfma(a[0], b[0], c[0]), sfma(a[1], b[1], c[1])}; }
DI f32x2 pmul(f32x2 a, f32x2 b) { return f32x2{smul(a[0], b[0]), smul(a[1], b[1])}; }
DI f32x2 cfma(f32x2 a, f32x2 ai, f32x2 z, f32x2 u) { return pfma(a, f32x2{z[0], z[0]}, pfma(ai, f32x2{z[1], z[1]}, u)); }
DI f32x2 csq(f32x2 a) { return __builtin_elementwise_fma(a, f32x2{a[0], a[0]}, rot90(a) * f32x2{a[1], a[1]}); }
DI float rowsum(float v) {
  const auto a = __builtin_amdgcn_permlane16_swap(__float_as_uint(v), __float_as_uint(v), false, false);
  const float s = __uint_as_float(a[0]) + __uint_as_float(a[1]);
  const auto c = __builtin_amdgcn_permlane32_swap(__float_as_uint(s), __float_as_uint(s), false, false);
  return __uint_as_float(c[0]) + __uint_as_float(c[1]);
}
DI f32x4 rowgather(float v) {
  const auto a = __builtin_amdgcn_permlane16_swap(__float_as_uint(v), __float_as_uint(v), false, false);
  const auto e = __builtin_amdgcn_permlane32_swap(a[0], a[0], false, false);
  const auto o = __builtin_amdgcn_permlane32_swap(a[1], a[1], false, false);
  return f32x4{__uint_as_float(e[0]), __uint_as_float(o[0]), __uint_as_float(e[1]), __uint_as_float(o[1])};
}
DI f32x2 cmul(f32x2 a, f32x2 z) { return pfma(a, f32x2{z[0], z[0]}, pmul(rot90(a), f32x2{z[1], z[1]})); }
DI unsigned cvtpk_neg(float lo, float hi) { unsigned r; asm("v_cvt_pk_bf16_f32 %0, -%1, -%2" : "=v"(r) : "v"(lo), "v"(hi)); return r; }

#define STAMP(i)

#define CSTAMP(i)
__global__ void __launch_bounds__(512, 2)
fused_ssm_mqa_kernel(const unsigned short* __restrict__ xa, const unsigned short* __restrict__ lre, const unsigned short* __restrict__ lim,
                     const unsigned short* __restrict__ bre, const unsigned short* __restrict__ bim,
                     const unsigned short* __restrict__ cre, const unsigned short* __restrict__ cim,
                     const unsigned short* __restrict__ ldt, const unsigned short* __restrict__ dsk,
                     const unsigned short* __restrict__ xb, const unsigned short* __restrict__ wq,
                     const unsigned short* __restrict__ wk, const unsigned short* __restrict__ wv,
                     const unsigned short* __restrict__ wo, float* __restrict__ out, char* __restrict__ wsp) {
  __shared__ __attribute__((aligned(16))) char smem[163840];
  const unsigned LB = (unsigned)(uintptr_t)(LDSP char*)smem;
  int lane = threadIdx.x & 63;
  const int w = __builtin_amdgcn_readfirstlane(threadIdx.x >> 6);
  const int b = blockIdx.x, rb = (b & 7) * 4 + (b >> 6), j = (b >> 3) & 7;
  const int t0 = rb * 64;
  auto cp = [&]() {
    unsigned long long pc; unsigned len;
    asm volatile("s_getpc_b64 %0\nKBEG%=:\n\ts_mov_b32 %1, ssm_mqa_code_end-KBEG%=" : "=s"(pc), "=s"(len));
    touch(mk((const void*)pc, len), lane < 12 ? ((b >> 3) + 32 * lane) * 128u : OOB, LB + 163328);
  };
  if (CP == 6 && w == 6) cp();
  if (CP == 7) {
    unsigned long long pc; unsigned len;
    asm volatile("s_getpc_b64 %0\nKBEG%=:\n\ts_mov_b32 %1, ssm_mqa_code_end-KBEG%=" : "=s"(pc), "=s"(len));
    const unsigned u = threadIdx.x - 384;
    touch(mk((const void*)pc, len), u < 12 ? ((b >> 3) + 32 * u) * 128u : OOB, LB + 163328);
    __builtin_amdgcn_sched_barrier(0);
  }
  asm volatile("" :: "s"(xa), "s"(lre), "s"(lim), "s"(bre), "s"(bim), "s"(cre), "s"(cim), "s"(ldt), "s"(dsk), "s"(xb), "s"(wq), "s"(wk), "s"(wv), "s"(wo), "s"(out), "s"(wsp));
  int r32 = lane & 31, h = lane >> 5, r16 = lane & 15, g = lane >> 4, q4 = (lane & 15) >> 2, p4 = lane & 3;
  const rsrc_t ws = mk(wsp, WS_END);
  const rsrc_t rxb = mk(xb, 2048 * 1024);
  STAMP(0);

  const int kind = j & 1, sub = j >> 1, rot = rb & 3;
  const unsigned C0 = LB + L_CNT, C1 = C0 + 4, C2 = C0 + 8, C3 = C0 + 12, C4 = C0 + 16, C5 = C0 + 20, CE = C0 + 24, C6 = C0 + 44;
  const unsigned vrow4 = g * 16384 + r16 * 16;
  const unsigned vrowc = g * 1024 + ((r16 ^ (4 * g)) * 16);
  if (w < 4) {
    const rsrc_t rkv = mk(kind ? wv : wk, 512 * 32);
    const int gq = (w + rot) & 3;
#pragma unroll
    for (int n = 0; n < 4; ++n) {
      dma(rxb, vrowc, (t0 + 16 * sub + 4 * n) * 1024 + gq * 256, LB + L_XB + w * L_OP + (4 * sub + n) * 1040);
      dma(rkv, lane * 16, (4 * gq + n) * 1024, LB + L_WKV + (4 * w + n) * 1024);
    }
  }
  auto tl = [&]() {
    const void* p = j == 0 ? (const void*)xa : j == 1 ? bre : j == 2 ? bim : j == 3 ? cre : j == 4 ? cim : j == 5 ? wo : j == 6 ? (const void*)out : (const void*)wsp;
    const unsigned n = (j == 0 ? 1 : j < 6 ? 2 : 4) << DS;
    const unsigned o = j == 0 ? t0 * 1024 : j < 6 ? rot * 131072 : j == 6 ? t0 * 4096 : WS_H + rb * 131072;
    touch(mk(p, 8 << 20), lane >= n ? OOB : (j == 7 && (lane >> DS) >= 2 ? ((lane >> DS) == 2 ? WS_O + rb * 65536 - 131072 : WS_E + (rot & 1) * 65536 - 196608) : o) + lane * (65536 >> DS), LB + 163328);
  };
  if (TLB == 1 && w == 7) tl();
  if (CP == 1 && w == 6) cp();
  if (w == 5 && lane < 12) sts32(C0 + lane * 4, 0u);
  if (WT == 1 && w == 7) touch(ws, lane == 0 ? WS_KV : OOB, LB + 163328);
  unsigned short r_lr = 0, r_li = 0, r_dt = 0, r_ds = 0;
  if (SL == 1 && w == 5) { r_lr = lre[64 * j + lane]; r_li = lim[64 * j + lane]; r_dt = ldt[64 * j + lane]; r_ds = dsk[64 * j + lane]; sb0(); }
  if (w != 4) hbar();
  if (WT == 2 && w == 7) touch(ws, lane == 0 ? WS_KV : OOB, LB + 163328);
  if (TLB == 3 && w == 7) tl();
  if (CP == 3 && w == 6) cp();
  unsigned e0, e1;
  u32x4 bf[8][4];
  auto bfload = [&]() {
    const unsigned rowl = 256 * (w >> 2) + 16 * (g >> 1) + 4 * (g & 1) + q4;
#pragma unroll
    for (int nb4 = 0; nb4 < 4; ++nb4) {
      const unsigned a = LB + rowl * 128 + ((nb4 ^ ((rowl >> 1) & 3)) * 32) + p4 * 8;
#pragma unroll
      for (int hp = 0; hp < 8; ++hp) bf[hp][nb4] = ldstr2(a + hp * 4096, a + hp * 4096 + 1024);
    }
  };
  if (w < 4) {
    {
    vmwait<0>();
    STAMP(1);
    f32x4 acc = {0, 0, 0, 0};
    const unsigned ax = LB + L_XB + w * L_OP + (4 * sub + q4) * 1040 + p4 * 256 + g * 16;
    const unsigned aw = LB + L_WKV + w * 4096 + (8 * g + q4) * 32 + 8 * p4;
    u32x4 fx[4], fw[4];
#pragma unroll
    for (int ks = 0; ks < 4; ++ks) { fx[ks] = lds128(ax + (ks ^ p4) * 64); fw[ks] = ldstr2(aw + ks * 1024, aw + ks * 1024 + 128); }
    if (kind == 0) {
#pragma unroll
      for (int ks = 0; ks < 4; ++ks) acc = mfma16(fw[ks], fx[ks], acc);
    } else {
#pragma unroll
      for (int ks = 0; ks < 4; ++ks) acc = mfma16(fx[ks], fw[ks], acc);
    }
    sts128(LB + L_KP + w * 1024 + lane * 16, asu4(acc));
    cnt_add(C4, lane);
    STAMP(2);
    }
    const int nb = w & 1, tb = w >> 1;
    u32x4 qf[8];
    {
      f32x16 acc = {};
      const unsigned ax = LB + L_XB + (8 * tb + (r32 >> 2)) * 1040 + p4 * 256 + h * 16;
      const unsigned aw = LB + L_WQ + (8 * h + q4) * 128 + ((nb ^ (q4 >> 1)) * 64) + (g & 1) * 32 + p4 * 8;
      cnt_wait<0>(C4, 5);
      const u32x2 ev = *(const LDSP u32x2*)(uintptr_t)CE;
      e0 = __builtin_amdgcn_readfirstlane(ev[0]); e1 = __builtin_amdgcn_readfirstlane(ev[1]);
      u32x4 fx[8], fw[8];
      auto xrd = [&](int c, int ks) { fx[ks] = lds128(ax + ((ks >> 1) ^ p4) * 64 + c * L_OP + (ks & 1) * 32); };
      sfor<4>([&](auto I) {
        constexpr int c = I.v;
        cnt_wait<0>(c ? C1 + 24 + 4 * c : C1, 3);
        if (c == 3) STAMP(3);
#pragma unroll
        for (int ks = 0; ks < 8; ++ks) { fw[ks] = ldstr2(aw + (8 * c + ks) * 2048, aw + (8 * c + ks) * 2048 + 512); if (c == 0) xrd(0, ks); }
#pragma unroll
        for (int ks = 0; ks < 8; ++ks) { acc = mfma32(fw[ks], fx[ks], acc); if (c < 3) xrd(c + 1, ks); }
      });
#pragma unroll
      for (int u = 0; u < 2; ++u) {
#pragma unroll
        for (int i = 0; i < 4; ++i) qf[u][i] = cvtpk(acc[8 * u + 2 * i], acc[8 * u + 2 * i + 1]);
        sts128(LB + L_QX + w * 2048 + u * 1024 + lane * 16, qf[u]);
      }
    }
    cnt_add(C2, lane);
    STAMP(4);
    cnt_wait<0>(C3, 1);
    const rsrc_t rk = mk(wsp + WS_KV + w * 16 * TS, 16 * TS);
    const unsigned vv = r32 < 16 ? (unsigned)((h * 16 + r32) * 16) : (r32 == 16 ? 512u : OOB);
    u32x4 kv[3];
    unsigned so, cnt;
    asm volatile(ATTN_PRO
                 : "={v[192:195]}"(kv[0]), "={v[196:199]}"(kv[1]), "={v[200:203]}"(kv[2])
                 : "{v240}"(lane * 16), "{v241}"(vv), [srd] "s"(rk) : "memory");
    cnt_wait<0>(C2, 4);
#pragma unroll
    for (int f = 2; f < 8; ++f) qf[f] = lds128(LB + L_QX + (w ^ (f >> 1)) * 2048 + (f & 1) * 1024 + lane * 16);
    asm volatile("s_waitcnt lgkmcnt(0)" : "+v"(qf[2]), "+v"(qf[3]), "+v"(qf[4]), "+v"(qf[5]), "+v"(qf[6]), "+v"(qf[7]) :: "memory");
    STAMP(5); CSTAMP(14);
    f32x16 o0, o1, ox_[6];
    {
      asm volatile(ATTN_LOOP
                   : "={v[32:47]}"(o0), "={v[48:63]}"(o1), "={v[64:79]}"(ox_[0]), "={v[80:95]}"(ox_[1]), "={v[96:111]}"(ox_[2]), "={v[112:127]}"(ox_[3]),
                     "={v[128:143]}"(ox_[4]), "={v[144:159]}"(ox_[5]), [so] "=&s"(so), [cnt] "=&s"(cnt),
                     "+{v[192:195]}"(kv[0]), "+{v[196:199]}"(kv[1]), "+{v[200:203]}"(kv[2])
                   : "{v[160:163]}"(qf[0]), "{v[164:167]}"(qf[1]), "{v[168:171]}"(qf[2]), "{v[172:175]}"(qf[3]),
                     "{v[176:179]}"(qf[4]), "{v[180:183]}"(qf[5]), "{v[184:187]}"(qf[6]), "{v[188:191]}"(qf[7]),
                     "{v240}"(lane * 16), "{v241}"(vv), "{v242}"(0xDDDD), [srd] "s"(rk), [c6] "s"(C6)
                   : ATTN_CLOB, "scc", "memory");
    }
    STAMP(6); CSTAMP(15);
    asm volatile("v_mbcnt_lo_u32_b32 %0, -1, 0\n\tv_mbcnt_hi_u32_b32 %0, -1, %0" : "=v"(lane));
    r32 = lane & 31; h = lane >> 5; r16 = lane & 15; g = lane >> 4; q4 = (lane & 15) >> 2; p4 = lane & 3;
    const unsigned voffV = r32 < 16 ? (unsigned)((h * 16 + r32) * 16) : (r32 == 16 ? 512u : OOB);
    cnt_wait<1>(C0, 16);
    if (PR & 1) __builtin_amdgcn_s_setprio(3);
#pragma unroll
    for (int f = 0; f < 6; ++f) {
      const unsigned ox = LB + L_OX + (w ^ ((f >> 1) + 1)) * 13824 + f * 2304;
      const f32x16& o = ox_[f];
      sts128(ox + lane * 16, asu4(f32x4{o[0], o[1], o[2], o[3]}));
      sts128(ox + 1024 + lane * 16, asu4(f32x4{o[4], o[5], o[6], o[7]}));
      sts32(ox + 2048 + lane * 4, __float_as_uint(o[8]));
    }
    const float lown0 = __shfl(o0[8], r32), lown1 = __shfl(o1[8], r32);
    sbar(C4, 9, lane);
    STAMP(7);
    {
      const unsigned ox = LB + L_OX + w * 13824;
      float ov[2][8], l[2];
      bool bad = false;
#pragma unroll
      for (int u = 0; u < 2; ++u) {
        const f32x16& o = u ? o1 : o0;
        f32x4 a = {o[0], o[1], o[2], o[3]}, c = {o[4], o[5], o[6], o[7]};
        l[u] = u ? lown1 : lown0;
#pragma unroll
        for (int x = 0; x < 3; ++x) {
          a += asf4(lds128(ox + (2 * x + u) * 2304 + lane * 16)); c += asf4(lds128(ox + (2 * x + u) * 2304 + 1024 + lane * 16));
          l[u] += __uint_as_float(*(const LDSP unsigned*)(uintptr_t)(ox + (2 * x + u) * 2304 + 2048 + r32 * 4));
        }
#pragma unroll
        for (int i = 0; i < 4; ++i) { ov[u][i] = a[i]; ov[u][4 + i] = c[i]; }
        const f32x4 t = a + c;
        const float am = fabsf((t[0] + t[1]) + (t[2] + t[3]));
        bad = bad || !(l[u] >= 0x1p-80f && l[u] <= 0x1p80f && am <= 0x1p120f);
      }
      if (__builtin_amdgcn_ballot_w64(bad) != 0) {
        const rsrc_t rk = mk(wsp + WS_KV, 64 * TS);
#pragma unroll 1
        for (int u = 0; u < 2; ++u) {
          const u32x4 qq = u ? qf[1] : qf[0];
          f32x16 a = {};
          float m = -INFINITY;
#pragma unroll 1
          for (int t = 0; t < 64; ++t) {
            const u32x4 kf = __builtin_amdgcn_raw_buffer_load_b128(rk, lane * 16, t * TS, 16);
            const u32x4 va = __builtin_amdgcn_raw_buffer_load_b128(rk, voffV, t * TS + 1024, 16);
            const u32x4 vb = __builtin_amdgcn_raw_buffer_load_b128(rk, voffV, t * TS + 1568, 16);
            f32x16 s = mfma32(kf, qq, f32x16{});
            float mx = s[0];
#pragma unroll
            for (int i = 1; i < 16; ++i) mx = fmaxf(mx, s[i]);
            mx = fmaxf(mx, __shfl_xor(mx, 32));
            const float mn = fmaxf(m, mx);
            const float al = __builtin_amdgcn_exp2f(m - mn);
            m = mn;
            f32x8 p0, p1;
#pragma unroll
            for (int i = 0; i < 8; ++i) { p0[i] = __builtin_amdgcn_exp2f(s[i] - mn); p1[i] = __builtin_amdgcn_exp2f(s[8 + i] - mn); }
            a *= al;
            a = __builtin_amdgcn_smfmac_f32_32x32x32_bf16(asbf(va), __builtin_bit_cast(bf16x16, p0), a, 0xDDDD, 0, 0);
            a = __builtin_amdgcn_smfmac_f32_32x32x32_bf16(asbf(vb), __builtin_bit_cast(bf16x16, p1), a, 0xDDDD, 0, 0);
          }
          const float ls = __shfl(a[8], r32);
          if (u == 0) {
            l[0] = ls;
#pragma unroll
            for (int i = 0; i < 8; ++i) ov[0][i] = a[i];
          } else {
            l[1] = ls;
#pragma unroll
            for (int i = 0; i < 8; ++i) ov[1][i] = a[i];
          }
        }
      }
#pragma unroll
      for (int u = 0; u < 2; ++u) {
        const float inv = __builtin_amdgcn_rcpf(l[u]);
        u32x4 pk;
#pragma unroll
        for (int i = 0; i < 4; ++i) pk[i] = min(cvtpk(ov[u][2 * i] * inv, ov[u][2 * i + 1] * inv), POISON - 1u);
        const unsigned off = WS_O + rb * OSZ + ((2 * tb + (r32 >> 4)) * 16 + 2 * j + nb) * 1024 + ((2 * u + h) * 16 + r16) * 16;
        __builtin_amdgcn_raw_buffer_store_b128(pk, ws, off, 0, 16);
      }
      if (b == 0 && w == 0 && lane == 0) __builtin_amdgcn_raw_buffer_store_b64(u32x2{e0, e1}, ws, WS_EPOCH, 0, 16);
    }
    cnt_add(C3, lane);
    STAMP(8);
    if (PR & 1) __builtin_amdgcn_s_setprio(0);
    cnt_wait<0>(C5, 5);
    bfload();
  } else {
    const int s4 = w - 4, tb = s4 & 1, pb = s4 >> 1;
    const rsrc_t rxa = mk(xa, 2048 * 1024);
    {
    const rsrc_t rq = mk(wq, 512 * 1024);
    const unsigned vq = (lane >> 3) * 1024 + (((lane & 7) ^ (((lane >> 4) & 1) << 2)) * 16) + j * 128;
    if (w == 4) {
      __builtin_amdgcn_s_setprio(3);
      u32x2 ep;
      asm volatile("s_load_dwordx2 %0, %1, 0x0\n\ts_barrier\n\t"
                   "s_add_u32 m0, %4, 0\n\ts_nop 0\n\tbuffer_load_dwordx4 %2, %3, %5 offen lds\n\t"
                   "s_add_u32 m0, %4, 0x4000\n\ts_nop 0\n\tbuffer_load_dwordx4 %2, %3, %6 offen lds\n\t"
                   "s_add_u32 m0, %4, 0x8000\n\ts_nop 0\n\tbuffer_load_dwordx4 %2, %3, %7 offen lds\n\t"
                   "s_add_u32 m0, %4, 0xc000\n\ts_nop 0\n\tbuffer_load_dwordx4 %2, %3, %8 offen lds\n\t"
                   "s_waitcnt lgkmcnt(0)"
                   : "=&s"(ep) : "s"(wsp + WS_EPOCH), "v"(vq), "s"(rq), "s"(LB + L_WQ + 15 * 1024), "s"((16 * rot + 15) * 8192), "s"((16 * ((1 + rot) & 3) + 15) * 8192),
                     "s"((16 * ((2 + rot) & 3) + 15) * 8192), "s"((16 * ((3 + rot) & 3) + 15) * 8192) : "m0", "scc", "memory");
      e0 = ep[0] + 1u; e1 = ep[1] ^ 0x5bd1e995u;
      if (lane == 0) sts64(CE, u32x2{e0, e1});
      vmwait<0>();
      cnt_wait<0>(C4, 4);
      f32x4 acc = asf4(lds128(LB + L_KP + lane * 16));
#pragma unroll
      for (int i = 1; i < 4; ++i) acc += asf4(lds128(LB + L_KP + i * 1024 + lane * 16));
      if (kind == 0) acc *= 0.25f * 1.4426950408889634f;
      const u32x2 pk = {cvtpk(acc[0], acc[1]), cvtpk(acc[2], acc[3])};
      const unsigned tb0 = WS_KV + (rb * 2 + (sub >> 1)) * TS;
      STAMP(1);
      if (kind == 0) {
        const auto a0 = __builtin_amdgcn_permlane32_swap(pk[0], pk[0], false, false), a1 = __builtin_amdgcn_permlane32_swap(pk[1], pk[1], false, false);
        __builtin_amdgcn_raw_buffer_store_b128(u32x4{a0[0], a1[0], a0[1], a1[1]}, ws, h ? OOB : (unsigned)((g * 32 + r16) * 16), tb0 + (sub & 1) * 256, 16);
      } else {
        const auto a0 = __builtin_amdgcn_permlane16_swap(pk[0], pk[0], false, false), a1 = __builtin_amdgcn_permlane16_swap(pk[1], pk[1], false, false);
        const u32x4 ones = {0x3f803f80u, 0x3f803f80u, 0x3f803f80u, 0x3f803f80u};
        __builtin_amdgcn_raw_buffer_store_b128(g & 1 ? ones : u32x4{a0[0], a1[0], a0[1], a1[1]}, ws, g & 1 ? (lane == 16 ? 512u : OOB) : (unsigned)((h * 16 + r16) * 16),
                                               tb0 + 1024 + (sub & 1) * 544, 16);
      }
      __builtin_amdgcn_raw_buffer_store_b64(u32x2{POISON, POISON}, ws, lane * 8, WS_E + (rb * 512 + 64 * j) * 8, 16);
      cnt_add(C4, lane);
      vmwait<0>();
      if (lane < 8) __builtin_amdgcn_raw_buffer_store_b64(u32x2{e0, e1}, ws, WS_FKV + lane * 2048 + (rb * 8 + j) * 8, 0, 16);
      STAMP(2);
      cnt_wait<2>(C1 + 36, 3);
      poll32(ws, WS_FKV + (b & 7) * 2048 + lane * 16, e0, e1);
      cnt_add(C3, lane);
      STAMP(3);
      __builtin_amdgcn_s_setprio(0);
    } else {
      const int wp = w - 5;
      unsigned gr[4], lr[4];
#pragma unroll
      for (int n = 0; n < 4; ++n) { const int r = (4 * sub + 4 + 4 * wp + n) & 15; gr[n] = (t0 + 4 * r) * 1024; lr[n] = LB + L_XB + r * 1040; }
      auto quarter = [&](auto I) {
        constexpr int c = I.v;
        const int gq = (c + rot) & 3;
        const unsigned gw = (16 * gq + 5 * wp) * 8192, lw = LB + L_WQ + 5 * wp * 1024;
        sfor<5>([&](auto N) {
          constexpr int n = N.v;
          dma2<n * 8192, (16 * c + n) * 1024>(rq, vq, gw, lw);
          if constexpr (n < 4 && c < 3) dma2<0, (c + 1) * L_OP>(rxb, vrowc + ((gq + 1) & 3) * 256, gr[n], lr[n]);
        });
      };
      unsigned d0 = 0, d1 = 0;
      if (ST && w == 7) {
        const char* pa = j == 0 ? (const char*)xa + t0 * 1024 : j == 6 ? (const char*)out + t0 * 4096 : j == 7 ? wsp + WS_H + rb * HS + 131072 :
                         (const char*)(j == 1 ? bre : j == 2 ? bim : j == 3 ? cre : j == 4 ? cim : wo) + rot * 131072;
        const char* pb_ = j < 4 ? (const char*)(j == 0 ? lre : j == 1 ? lim : j == 2 ? ldt : dsk) : j == 4 ? wsp + WS_O + rb * OSZ - 128 : j == 5 ? wsp + WS_O + rb * OSZ + 65536 : j == 6 ? pa + 131072 : pa - HS;
        asm volatile("s_load_dword %0, %2, 0x0\n\ts_load_dword %1, %3, 0x0" : "={s96}"(d0), "={s97}"(d1) : "s"(pa), "s"(pb_));
      }
      sfor<4>([&](auto N) { dma2<0, 0>(rxb, vrowc + rot * 256, gr[N.v], lr[N.v]); });
      quarter(IC<0>{});
      if (SL == 2 && w == 5) { sb0(); r_lr = lre[64 * j + lane]; r_li = lim[64 * j + lane]; r_dt = ldt[64 * j + lane]; r_ds = dsk[64 * j + lane]; sb0(); }
      if (TLB == 4 && w == 7) tl();
      if (CP == 4 && w == 6) cp();
      quarter(IC<1>{});
      vmwait<9>(); cnt_add(C1, lane);
      if (ST) asm volatile("" :: "{s96}"(d0), "{s97}"(d1));
      if (TLB == 5 && w == 7) tl();
      if (CP == 5 && w == 6) cp();
      quarter(IC<2>{});
      vmwait<9>(); cnt_add(C1 + 28, lane);
      quarter(IC<3>{});
      vmwait<5>(); cnt_add(C1 + 32, lane);
      if (SL == 0 && w == 5) {
        sb0(); r_lr = lre[64 * j + lane]; r_li = lim[64 * j + lane]; r_dt = ldt[64 * j + lane]; r_ds = dsk[64 * j + lane]; sb0();
        vmwait<4>();
      } else if (TLB == 2 && w == 7) { tl(); vmwait<1>(); }
      else if (CP == 2 && w == 6) { cp(); vmwait<1>(); }
      else vmwait<0>();
      cnt_add(C1 + 36, lane);
      if (FT && w > 5) {
        __builtin_amdgcn_s_setprio(3);
        const unsigned v1 = lane == 0 ? 64 * j * 1024 : OOB, lt = LB + 163328;
        if (w == 6) { touch(rxa, lane == 0 ? t0 * 1024 : OOB, lt); touch(mk(bre, 524288), v1, lt); touch(mk(bim, 524288), v1, lt); touch(mk(cim, 524288), v1, lt); }
        else { touch(mk(cre, 524288), v1, lt); touch(mk(wo, 524288), lane < (8 >> (FT - 1)) ? (lane << (15 + FT)) : OOB, lt);
               touch(mk(out, 2048 * 4096), lane < (4 >> (FT - 1)) ? t0 * 4096 + (lane << (15 + FT)) : OOB, lt);
               touch(ws, lane < 2 ? WS_H + rb * 131072 + lane * 65536u : lane == 2 ? WS_O + rb * 65536u : OOB, lt); }
      }
      STAMP(1);
      if (w == 5) {
        __builtin_amdgcn_s_setprio(1);
        const float lr = bf2f(r_lr), li = bf2f(r_li);
        const float dt = __uint_as_float(cvtpk(expf(bf2f(r_dt)), 0.f) << 16);
        const float mag = expf(dt * lr);
        float sn, cs;
        sincosf(dt * li, &sn, &cs);
        const f32x2 lam = {mag * cs, mag * sn};
        const float den = 2.0f / (lr * lr + li * li), nr = lam[0] - 1.0f, ni = lam[1];
        const f32x2 coef = {(nr * lr + ni * li) * den, (ni * lr - nr * li) * den};
        const f32x2 l16 = csq(csq(csq(csq(lam)))), l32 = csq(l16), l64 = csq(l32), l512 = csq(csq(csq(l64))), l1k = csq(l512);
        const f32x2 t[10] = {lam, coef, f32x2{1.f, 0.f}, l16, l32, cmul(l32, l16), l64, l512, l1k, cmul(l1k, l512)};
        cnt_wait<0>(C4, 4);
#pragma unroll
        for (int e = 0; e < 10; ++e) sts64(LB + L_TB + e * 512 + lane * 8, __builtin_bit_cast(u32x2, t[e]));
        sts32(LB + L_TB + 5120 + lane * 4, (unsigned)r_ds << (16 * (lane & 1)));
        cnt_add(C5, lane);
        __builtin_amdgcn_s_setprio(0);
      }
      STAMP(2);
    }
    }
    const rsrc_t rbr = mk(bre, 512 * 1024), rbi = mk(bim, 512 * 1024);
    const int p0 = 64 * j + 16 * s4;
    unsigned gx[4], gb[4], lx[4];
#pragma unroll
    for (int c = 0; c < 4; ++c) {
      gx[c] = __builtin_amdgcn_readfirstlane((t0 + 4 * s4) * 1024 + ((c + j + rot) & 3) * 256); lx[c] = LB + 4 * s4 * 1040 + ((c + j) & 3) * L_OP;
      gb[c] = __builtin_amdgcn_readfirstlane(p0 * 1024 + ((c + rot) & 3) * 256);
    }
    const unsigned lb = LB + L_RING + s4 * L_OP;
    const int a8 = lane >> 3;
    unsigned vb0 = a8 * 1024 + (((lane & 7) ^ (a8 >> 1)) * 16), vb1 = (a8 + 8) * 1024 + (((lane & 7) ^ (a8 >> 1) ^ 4) * 16);
    unsigned ax = LB + ((r32 & 3) + 4 * (r32 >> 3)) * 1040 + ((r32 >> 2) & 1) * 512 + h * 16;
    unsigned ab[4];
#pragma unroll
    for (int ks = 0; ks < 4; ++ks) ab[ks] = lb + r32 * 128 + (((2 * ks + h) ^ (r16 >> 1)) * 16);
    unsigned vc = ((lane + 8 * j) & 63) * 16;
    asm volatile("" : "+v"(vb0), "+v"(vb1), "+v"(ax), "+v"(ab[0]), "+v"(ab[1]), "+v"(ab[2]), "+v"(ab[3]), "+v"(vc));
    __builtin_amdgcn_s_setprio(3);
    cnt_wait<1>(C6, 256);
    if (w != 4) {
      const u32x2 ev = *(const LDSP u32x2*)(uintptr_t)CE;
      e0 = __builtin_amdgcn_readfirstlane(ev[0]); e1 = __builtin_amdgcn_readfirstlane(ev[1]);
    }
    STAMP(4);
    unsigned gc[4], lc[4];
#pragma unroll
    for (int q = 0; q < 4; ++q) { gc[q] = (p0 + 4 * ((q + rot) & 3)) * 1024; lc[q] = LB + (16 * s4 + 4 * q) * 1040; }
    const rsrc_t rci = mk(cim, 512 * 1024), rcr = mk(cre, 512 * 1024);
    auto cdma = [&](rsrc_t rc, auto I, auto O) { constexpr int n = I.v; dma2<(n & 3) * 1024, (O.v + (n & 3)) * 1040>(rc, vc, gc[n >> 2], lc[n >> 2]); };
    f32x16 acc0 = {}, acc1 = {};
    u32x4 xf[2], dg[2];
    f32x2 lam, coef, l16, l64, mseg, mblk, lami, coefi, l16i, l64i, msegi, l2;
    {
      auto bdma = [&](auto C, auto M) {
        constexpr int c = C.v, m = M.v;
        dma2<(c & 1) * 128, (c & 3) * 4096 + m * 1024>(m & 2 ? rbi : rbr, m & 1 ? vb1 : vb0, gb[c >> 1], lb);
      };
      sfor<16>([&](auto I) { constexpr int n = I.v; dma2<(n & 3) * 1024, (n & 3) * 1040>(rxa, vrow4, gx[n >> 2], lx[n >> 2]); vmwait<TH>(); });
      sfor<4>([&](auto I) { bdma(IC<0>{}, I); vmwait<TH>(); });
      cnt_wait<1>(C5, 1);
      auto tbl = [&](int e) { return *(const LDSP f32x2*)(uintptr_t)(LB + L_TB + e * 512 + (16 * s4 + r16) * 8); };
      lam = tbl(0); coef = tbl(1); l16 = tbl(3); l64 = tbl(6);
      mseg = tbl(2 + g); mblk = tbl(g ? 6 + g : 2);
      if (8 * g >= rb) mblk = f32x2{0.f, 0.f};
      lami = rot90(lam); coefi = rot90(coef); l16i = rot90(l16); l64i = rot90(l64); msegi = rot90(mseg); l2 = csq(lam);
      {
        const int rp = (r32 & 16) | ((r32 + 4 * rot) & 15);
        const unsigned dv = *(const LDSP unsigned*)(uintptr_t)(LB + L_TB + 5120 + (32 * pb + rp) * 4);
#pragma unroll
        for (int k2 = 0; k2 < 2; ++k2)
#pragma unroll
          for (int m = 0; m < 4; ++m) dg[k2][m] = (rp >> 1) == 8 * k2 + 4 * h + m ? dv : 0u;
      }
      asm volatile("" : "+v"(lam), "+v"(lami), "+v"(coef), "+v"(coefi), "+v"(l16), "+v"(l16i), "+v"(l64), "+v"(l64i), "+v"(mseg), "+v"(msegi), "+v"(mblk), "+v"(l2));
      asm volatile("" : "+v"(dg[0]), "+v"(dg[1]));
      vmwait<4>();
      cnt_add(C0, lane);
      sfor<4>([&](auto I) { bdma(IC<1>{}, I); vmwait<TH>(); });
      cnt_wait<1>(C0, 4);
      u32x4 fx[8], fb[4];
#pragma unroll
      for (int q = 0; q < 8; ++q) fx[q] = lds128(ax + (q >> 1) * 32 + (q & 1) * 256);
      sfor<8>([&](auto I) {
        constexpr int c = I.v;
        if (c < 7) vmwait<4>(); else vmwait<7>();
#pragma unroll
        for (int ks = 0; ks < 4; ++ks) fb[ks] = lds128(ab[ks] + (c & 3) * 4096);
        sfor<4>([&](auto K) {
          constexpr int ks = K.v;
          if constexpr (c < 6) {
            if (ks) vmwait<TH>();
            bdma(IC<(c < 6 ? c + 2 : 0)>{}, IC<ks>{});
          } else if constexpr (c == 6) {
            vmwait<TH>(); cdma(rci, IC<2 * ks>{}, IC<64>{});
            if constexpr (ks < 3) { vmwait<TH>(); cdma(rci, IC<2 * ks + 1>{}, IC<64>{}); }
          } else {
            vmwait<TH>(); cdma(rci, IC<7 + ks>{}, IC<64>{});
          }
          sb0();
          mpair<X1>(acc0, fx[2 * ks], fb[ks], acc1, fx[2 * ks + 1], fb[ks]);
          sb0();
          if constexpr (c < 7) {
            constexpr int o = ((c + 1) >> 1) * L_OP + ((c + 1) & 1) * 128 + ks * 32;
            fx[2 * ks] = lds128(ax + o); fx[2 * ks + 1] = lds128(ax + o + 256);
          }
        });
      });
      const unsigned af = LB + (((j >> 1) - rot) & 3) * L_OP + r16 * 1040 + (2 * tb + (g & 1)) * 256 + (j & 1) * 128 + pb * 64 + h * 16;
      xf[0] = lds128(af); xf[1] = lds128(af + 32);
      cnt_add(C0, lane);
    }
    STAMP(5);
    f32x2 uu[16], br[8], bi[8], zr2, zi2;
    const float l2n = -l2[1];
    sfor<8>([&](auto I) {
      constexpr int i = I.v;
      if constexpr (i < 5) { sb0(); vmwait<TH>(); cdma(rci, IC<11 + i>{}, IC<64>{}); sb0(); }
      const auto s0 = __builtin_amdgcn_permlane16_swap(__float_as_uint(acc0[2 * i]), __float_as_uint(acc1[2 * i]), false, false);
      const auto s1 = __builtin_amdgcn_permlane16_swap(__float_as_uint(acc0[2 * i + 1]), __float_as_uint(acc1[2 * i + 1]), false, false);
      br[i] = f32x2{__uint_as_float(s0[0]), __uint_as_float(s1[0])};
      bi[i] = f32x2{__uint_as_float(s0[1]), __uint_as_float(s1[1])};
      if constexpr (i == 0) { zr2 = br[0]; zi2 = bi[0]; }
      else {
        const f32x2 a = {l2[0], l2[0]}, bb = {l2[1], l2[1]}, nb2 = {l2n, l2n};
        const f32x2 nr = pfma(a, zr2, pfma(nb2, zi2, br[i])), ni = pfma(a, zi2, pfma(bb, zr2, bi[i]));
        zr2 = nr; zi2 = ni;
      }
    });
    f32x2 zpre;
    {
      f32x2 z = cfma(lam, lami, f32x2{zr2[0], zi2[0]}, f32x2{zr2[1], zi2[1]});
      z = pfma(coefi, f32x2{z[1], z[1]}, pmul(coef, f32x2{z[0], z[0]}));
      const f32x4 zr = rowgather(z[0]), zi = rowgather(z[1]);
      const f32x2 h1 = {zr[0], zi[0]};
      const f32x2 h2 = cfma(l16, l16i, h1, f32x2{zr[1], zi[1]});
      const f32x2 h3 = cfma(l16, l16i, h2, f32x2{zr[2], zi[2]});
      const f32x2 E = cfma(l16, l16i, h3, f32x2{zr[3], zi[3]});
      __builtin_amdgcn_raw_buffer_store_b64(u32x2{min(__float_as_uint(E[0]), POISON - 1u), __float_as_uint(E[1])}, ws, g == 0 ? (unsigned)(r16 * 8) : OOB,
                                            WS_E + (rb * 512 + p0) * 8, 16);
      sb0();
      zpre = g == 0 ? f32x2{0.f, 0.f} : g == 1 ? h1 : g == 2 ? h2 : h3;
    }
    STAMP(6);
#pragma unroll
    for (int u = 0; u < 2; ++u)
      __builtin_amdgcn_raw_buffer_store_b128(u32x4{POISON, POISON, POISON, POISON}, ws, lane * 16, WS_O + rb * OSZ + (s4 * 16 + 2 * j + u) * 1024, 16);
    const rsrc_t ro = mk(wo, 512 * 1024);
    const unsigned vq = (lane >> 3) * 1024 + (((lane & 7) ^ (((lane >> 4) & 3) << 1)) * 16) + j * 128;
    u32x4 wr[16];
    auto wload = [&](auto I) { constexpr int n = I.v; sb0(); wr[n] = __builtin_amdgcn_raw_buffer_load_b128(ro, vq, (16 * s4 + 4 * ((rot + (n >> 2)) & 3) + (n & 3)) * 8192, 0); sb0(); };
    cnt_wait<1>(C0, 8);
    sfor<16>([&](auto I) {
      constexpr int i = I.v;
      uu[i] = pfma(coefi, f32x2{bi[i >> 1][i & 1], bi[i >> 1][i & 1]}, pmul(coef, f32x2{br[i >> 1][i & 1], br[i >> 1][i & 1]}));
      asm volatile("" : "+v"(uu[i])); vmwait<TH>(); cdma(rcr, I, IC<0>{}); if (PACE > 1) __builtin_amdgcn_s_sleep(PACE - 1);
    });
    f32x2 H;
    {
      const rsrc_t re_ = mk(wsp + WS_E, 32 * 512 * 8);
      u32x2 ev[8];
      const int k0 = ((rb - 1 - 8 * g) * 512 + p0 + r16) * 8;
#pragma unroll 1
      for (unsigned s = 0; s < SPIN_MAX; ++s) {
        asm volatile("" ::: "memory");
        unsigned mx = 0;
#pragma unroll
        for (int i = 0; i < 8; ++i) ev[i] = __builtin_amdgcn_raw_buffer_load_b64(re_, (unsigned)(k0 - i * 4096), 0, 16);
#pragma unroll
        for (int i = 0; i < 8; ++i) mx = max(mx, ev[i][0]);
        if (__builtin_amdgcn_ballot_w64(mx == POISON) == 0) break;
        __builtin_amdgcn_s_sleep(PS8);
      }
      STAMP(7);
      f32x2 a = __builtin_bit_cast(f32x2, ev[7]);
      sfor<7>([&](auto I) {
        constexpr int i = 6 - I.v;
        a = cfma(l64, l64i, a, __builtin_bit_cast(f32x2, ev[i]));
      });
      a = cmul(mblk, a);
      H = cfma(mseg, msegi, f32x2{rowsum(a[0]), rowsum(a[1])}, zpre);
    }
    sfor<16>([&](auto I) {
      constexpr int i = I.v;
      H = cfma(lam, lami, H, uu[i]); uu[i] = H;
    });
    {
      const unsigned tw = LB + L_TR + s4 * 4608 + r16 * 144 + g * 32;
      const unsigned trd = LB + L_TR + s4 * 4608 + (8 * h + q4) * 144 + (g & 1) * 32 + p4 * 8;
#pragma unroll
      for (int i = 0; i < 2; ++i) {
        sts128(tw + i * 16, u32x4{cvtpk(uu[8 * i][0], uu[8 * i + 1][0]), cvtpk(uu[8 * i + 2][0], uu[8 * i + 3][0]),
                                  cvtpk(uu[8 * i + 4][0], uu[8 * i + 5][0]), cvtpk(uu[8 * i + 6][0], uu[8 * i + 7][0])});
        sts128(tw + 2304 + i * 16, u32x4{cvtpk_neg(uu[8 * i][1], uu[8 * i + 1][1]), cvtpk_neg(uu[8 * i + 2][1], uu[8 * i + 3][1]),
                                         cvtpk_neg(uu[8 * i + 4][1], uu[8 * i + 5][1]), cvtpk_neg(uu[8 * i + 6][1], uu[8 * i + 7][1])});
      }
      asm volatile("s_waitcnt lgkmcnt(0)" ::: "memory");
#pragma unroll
      for (int q = 0; q < 4; ++q) {
        const unsigned a = trd + (q >> 1) * 64 + (q & 1) * 2304;
        __builtin_amdgcn_raw_buffer_store_b128(ldstr2(a, a + 4 * 144), ws, lane * 16, WS_H + rb * HS + (q * 32 + 4 * j + s4) * 1024, 16);
      }
    }
    vmwait<0>();
    STAMP(8);
    if (cnt_inc(C0, lane) == 11) {
      if (lane == 0) __builtin_amdgcn_raw_buffer_store_b64(u32x2{e0, e1}, ws, WS_FH + (rb * 8 + j) * 128, 0, 16);
      sfor<WN - WM>([&](auto I) { wload(I); __builtin_amdgcn_s_sleep(HD); });
      poll8(ws, lane < 8 ? (unsigned)(WS_FH + (rb * 8 + lane) * 128) : OOB, e0, e1);
      cnt_add(C3, lane);
    } else {
      sfor<WN - WM>([&](auto I) { wload(I); __builtin_amdgcn_s_sleep(HD); });
      cnt_wait<1>(C3, 2);
    }
    STAMP(9);
    f32x16 yk, ys = {};
    {
      u32x4 af[AD + 1];
      const unsigned hb = WS_H + rb * HS + (2 * tb + pb) * 32768;
#pragma unroll
      for (int ks = 0; ks < AD; ++ks) af[ks] = __builtin_amdgcn_raw_buffer_load_b128(ws, lane * 16 + (ks & 3) * 1024, hb + (((ks >> 2) + j) & 7) * 4096, 16);
      yk = mfma32(dg[1], xf[1], mfma32(dg[0], xf[0], f32x16{}));
      const unsigned ak = LB + (pb * 64 + pb * 32 + r32) * 1040 + h * 16, as = LB + (pb * 64 + (pb ^ 1) * 32 + r32) * 1040 + h * 16;
      u32x4 fk[4], fs[4];
#pragma unroll
      for (int ks = 0; ks < 4; ++ks) { fk[ks] = lds128(ak + ks * 32); fs[ks] = lds128(as + ks * 32); }
#pragma unroll
      for (int ks = 0; ks < 32; ++ks) {
        sb0();
        if (const int n = WN - WM + ks / (24 / (WM + !WM)); WM && ks % (24 / (WM + !WM)) == 0 && n < WN) wr[n] = __builtin_amdgcn_raw_buffer_load_b128(ro, vq, (16 * s4 + 4 * ((rot + (n >> 2)) & 3) + (n & 3)) * 8192, 0);
        if (const int n = ks + AD - 32 + WN; ks + AD >= 32 && n < 16) wr[n] = __builtin_amdgcn_raw_buffer_load_b128(ro, vq, (16 * s4 + 4 * ((rot + (n >> 2)) & 3) + (n & 3)) * 8192, 0);
        if (ks + AD < 32) af[(ks + AD) % (AD + 1)] = __builtin_amdgcn_raw_buffer_load_b128(ws, lane * 16 + ((ks + AD) & 3) * 1024, hb + ((((ks + AD) >> 2) + j) & 7) * 4096, 16);
        sb0();
        if (ks < XK) mpair<X2>(ys, fs[ks & 3], af[ks % (AD + 1)], yk, fk[ks & 3], af[ks % (AD + 1)]);
        else mpair<X3>(ys, fs[ks & 3], af[ks % (AD + 1)], yk, fk[ks & 3], af[ks % (AD + 1)]);
        sb0();
        const int kn = ks + 4;
        if (kn < 32) { fk[ks & 3] = lds128(ak + kn * 32); fs[ks & 3] = lds128(as + kn * 32); }
      }
    }
#pragma unroll
    for (int n = AD + WN; n < 16; ++n) wr[n] = __builtin_amdgcn_raw_buffer_load_b128(ro, vq, (16 * s4 + 4 * ((rot + (n >> 2)) & 3) + (n & 3)) * 8192, 0);
    STAMP(10);
    sbar(C0, 16, lane);
    if (PR & 2) __builtin_amdgcn_s_setprio(PR >> 3);
    {
      const unsigned wb = LB + L_R2 + pb * 17408 + (32 * tb + r32) * 272;
#pragma unroll
      for (int i = 0; i < 4; ++i) {
        const unsigned wa = wb + ((2 * (i & 1) + h + rot) & 3) * 16 + (i >> 1) * 64;
        sts128(wa + pb * 128, asu4(f32x4{yk[4 * i], yk[4 * i + 1], yk[4 * i + 2], yk[4 * i + 3]}));
        sts128(wa + (pb ^ 1) * 128, asu4(f32x4{ys[4 * i], ys[4 * i + 1], ys[4 * i + 2], ys[4 * i + 3]}));
      }
    }
    cnt_add(C0, lane);
    auto wput = [&](int n) { sts128(LB + (16 * s4 + 4 * ((rot + (n >> 2)) & 3) + (n & 3)) * 1024 + lane * 16, wr[n]); };
#pragma unroll
    for (int n = 0; n < WN; ++n) wput(n);
    cnt_wait<1>(C0, 20);
    {
      const rsrc_t rout = mk(out, 2048 * 4096);
      const unsigned ra = LB + L_R2 + (16 * s4 + g) * 272 + r16 * 16;
#pragma unroll
      for (int n = 0; n < 4; ++n) {
        const f32x4 v = asf4(lds128(ra + n * 1088)) + asf4(lds128(ra + n * 1088 + 17408));
        __builtin_amdgcn_raw_buffer_store_b128(asu4(v), rout, g * 4096 + r16 * 16, (t0 + 16 * s4 + 4 * n) * 4096 + 256 * j, 16);
      }
    }
#pragma unroll
    for (int n = WN; n < 16; ++n) wput(n);
    cnt_add(C5, lane);
    STAMP(11);
    cnt_wait<1>(C5, 5);
    if (PR & 4) cnt_wait<1>(C4, 9);
    bfload();
    __builtin_amdgcn_s_setprio(0);
    if (OD) { cnt_wait<1>(C3, 6); __builtin_amdgcn_s_sleep(OD > 1 ? OD : 0); }
  }

  {
    const int t16 = w & 3, kh = w >> 2;
    u32x4 af[8];
    const unsigned ob = WS_O + rb * OSZ + (t16 * 16 + 8 * kh) * 1024;
#pragma unroll 1
    for (unsigned s = 0; s < SPIN_MAX; ++s) {
      asm volatile("" ::: "memory");
      unsigned mx = 0;
#pragma unroll
      for (int hp = 0; hp < 8; ++hp) af[hp] = __builtin_amdgcn_raw_buffer_load_b128(ws, lane * 16, ob + hp * 1024, 16);
#pragma unroll
      for (int hp = 0; hp < 8; ++hp) mx = max(max(mx, max(af[hp][0], af[hp][1])), max(af[hp][2], af[hp][3]));
      if (__builtin_amdgcn_ballot_w64(mx == POISON) == 0) break;
    }
    STAMP(12);
    f32x4 c[4] = {};
#pragma unroll
    for (int hp = 0; hp < 8; ++hp)
#pragma unroll
      for (int nb4 = 0; nb4 < 4; ++nb4) c[nb4] = mfma16(bf[hp][nb4], af[hp], c[nb4]);
    {
      const unsigned wa = LB + L_R2 + kh * 17408 + (16 * t16 + r16) * 272 + g * 16;
#pragma unroll
      for (int nb4 = 0; nb4 < 4; ++nb4) sts128(wa + nb4 * 64, asu4(c[nb4]));
    }
    hbar();
    const rsrc_t rout = mk(out, 2048 * 4096);
    const unsigned ra = LB + L_R2 + (8 * w + g) * 272 + r16 * 16;
#pragma unroll
    for (int u = 0; u < 2; ++u) {
      const f32x4 v = asf4(lds128(ra + u * 1088)) + asf4(lds128(ra + u * 1088 + 17408));
      __builtin_amdgcn_raw_buffer_store_b128(asu4(v), rout, g * 4096 + r16 * 16, (t0 + 8 * w + 4 * u) * 4096 + 2048 + 256 * j, 16);
    }
    asm volatile("ssm_mqa_code_end:");
    STAMP(13);
  }
}

extern "C" void kernel_launch(void* const* d_in, const int* in_sizes, int n_in, void* d_out, int out_size, void* d_ws, size_t ws_size,
                              hipStream_t stream) {
  typedef const unsigned short* P;
  fused_ssm_mqa_kernel<<<256, 512, 0, stream>>>((P)d_in[0], (P)d_in[1], (P)d_in[2], (P)d_in[3], (P)d_in[4], (P)d_in[5], (P)d_in[6],
                                               (P)d_in[7], (P)d_in[8], (P)d_in[9], (P)d_in[10], (P)d_in[11], (P)d_in[12], (P)d_in[13],
                                               (float*)d_out, (char*)d_ws);
}
```
